```python
import torch

def _softmax(x, axis=-1):
    return torch.softmax(x, dim=axis)
def _gelu(x):
    return torch.nn.functional.gelu(x, approximate="tanh")
def _silu(x):
    return torch.nn.functional.silu(x)
def _sigmoid(x):
    return torch.sigmoid(x)
def _layernorm(x, axis=-1, eps=1e-5):
    m = x.mean(dim=axis, keepdim=True)
    v = x.var(dim=axis, keepdim=True, unbiased=False)
    return (x - m) / torch.sqrt(v + eps)
def _scatter_add(tgt, idx, src, axis=0):
    return tgt.clone().index_add_(axis, idx, src)
def _segment_sum(src, seg, n):
    out = torch.zeros((n,) + src.shape[1:], dtype=src.dtype)
    return out.index_add_(0, seg, src)
def _pad(x, pw):
    flat = tuple(v for pair in reversed(pw) for v in pair)
    return torch.nn.functional.pad(x, flat)

kernel_name = 'sequential_preLN__sliding_local_dilated__dense_activation_silu__prompt'

def setup_inputs():

    _w = (lambda a: (lambda t: t.to(torch.bfloat16) if t.is_floating_point() else t)(torch.as_tensor(a)))
    def _A_setup():
        B, S, D = 1, 2048, 640
        return {
            "x": _w((lambda *s: torch.randn(*s, dtype=torch.bfloat16) * 0.05)(B, S, D)),
            "wqkv": _w((lambda *s: torch.randn(*s, dtype=torch.bfloat16) * 0.05)(D, 3 * D)),
            "wo": _w((lambda *s: torch.randn(*s, dtype=torch.bfloat16) * 0.05)(D, D)),
        }
    _A_in = _A_setup()

    def _B_setup():
        B, S, D, F = 1, 2048, 640, 1728
        return {
            "x": _w((lambda *s: torch.randn(*s, dtype=torch.bfloat16) * 0.05)(B, S, D)),
            "w1": _w((lambda *s: torch.randn(*s, dtype=torch.bfloat16) * 3.2)(D, F) * (D ** -0.5)),
            "w2": _w((lambda *s: torch.randn(*s, dtype=torch.bfloat16) * 0.4)(F, D) * (F ** -0.5)),
        }
    _B_in = _B_setup()

    return {
        "A_x": _A_in["x"],
        "A_wqkv": _A_in["wqkv"],
        "A_wo": _A_in["wo"],
        "B_x": _B_in["x"],
        "B_w1": _B_in["w1"],
        "B_w2": _B_in["w2"],
    }

def reference(A_x, A_wqkv, A_wo, B_x, B_w1, B_w2):
    def _A_ref(x, wqkv, wo):
        B, S, D = x.shape
        H = 32; Dh = D // H
        W, dil = 8, 2
        qkv = (lambda x, *d: x.permute(*d))((x @ wqkv).reshape(B, S, 3, H, Dh), 2, 0, 3, 1, 4)
        q, k, v = qkv[0], qkv[1], qkv[2]
        scale = Dh ** -0.5
        pos = torch.arange(S)
        off = torch.arange(W) * dil
        idx = pos[:, None] - off[None, :]
        mask = idx >= 0
        idx_c = torch.where(mask, idx, 0).reshape(-1)
        kg = k[:, :, idx_c, :].reshape(B, H, S, W, Dh)
        vg = v[:, :, idx_c, :].reshape(B, H, S, W, Dh)
        logits = torch.einsum("bhsd,bhswd->bhsw", q, kg) * scale
        logits = torch.where(mask[None, None, :, :], logits, -1e9)
        attn = _softmax(logits, axis=-1)
        o = torch.einsum("bhsw,bhswd->bhsd", attn, vg)
        return (lambda x, *d: x.permute(*d))(o, 0, 2, 1, 3).reshape(B, S, D) @ wo

    def _B_ref(x, w1, w2):
        h = torch.einsum("bsd,df->bsf", x, w1)
        h = _silu(h)
        out = torch.einsum("bsf,fd->bsd", h, w2)
        return out

    x = A_x
    h = x + _A_ref(_layernorm(x, axis=-1), A_wqkv, A_wo)
    h2 = h + _B_ref(_layernorm(h, axis=-1), B_w1, B_w2)
    return h2
```

```cpp
#include <hip/hip_runtime.h>
#include <hip/hip_bf16.h>
#include <stdint.h>

typedef unsigned short u16;
__device__ __forceinline__ float bf2f(u16 b){ return __uint_as_float(((unsigned)b)<<16); }
__device__ __forceinline__ u16 f2bf(float f){ __hip_bfloat16 h = __float2bfloat16(f); return *(u16*)&h; }

constexpr int S=2048, D=640, F=1728, H=32, DH=20;

__global__ void ln_k(const u16* x, u16* y){
  int r=blockIdx.x; int t=threadIdx.x;
  float v[10]; float s=0;
  for(int i=0;i<10;i++){ v[i]=bf2f(x[r*D+t+64*i]); s+=v[i]; }
  for(int o=32;o>0;o>>=1) s+=__shfl_xor(s,o);
  float m=s/D; float q=0;
  for(int i=0;i<10;i++){ float d=v[i]-m; q+=d*d; }
  for(int o=32;o>0;o>>=1) q+=__shfl_xor(q,o);
  float rs=rsqrtf(q/D+1e-5f);
  for(int i=0;i<10;i++) y[r*D+t+64*i]=f2bf((v[i]-m)*rs);
}
template<int MODE>
__global__ void gemm_k(const u16* A,const u16* B,u16* C,const u16* res,int N,int K){
  __shared__ float sa[16][17], sb[16][17];
  int tx=threadIdx.x&15, ty=threadIdx.x>>4;
  int row=blockIdx.y*16+ty, col=blockIdx.x*16+tx;
  float acc=0;
  for(int k0=0;k0<K;k0+=16){
    sa[ty][tx]=bf2f(A[row*K+k0+tx]);
    sb[ty][tx]=bf2f(B[(k0+ty)*N+col]);
    __syncthreads();
    for(int k=0;k<16;k++) acc+=sa[ty][k]*sb[k][tx];
    __syncthreads();
  }
  if(MODE==1){ float a=bf2f(f2bf(acc)); acc=a/(1.f+__expf(-a)); }
  if(MODE==2){ acc=bf2f(f2bf(acc))+bf2f(res[row*N+col]); }
  C[row*N+col]=f2bf(acc);
}
__global__ void attn_k(const u16* qkv,u16* o){
  int idx=blockIdx.x*blockDim.x+threadIdx.x; int s=idx/H, h=idx%H;
  float q[DH]; for(int d=0;d<DH;d++) q[d]=bf2f(qkv[s*3*D+h*DH+d]);
  float l[8]; float mx=-1e30f;
  for(int w=0;w<8;w++){ int j=s-2*w; float a=-1e9f; if(j>=0){ a=0; for(int d=0;d<DH;d++) a+=q[d]*bf2f(qkv[j*3*D+D+h*DH+d]); a*=0.22360679774997896f;} l[w]=a; mx=fmaxf(mx,a);}
  float sum=0; for(int w=0;w<8;w++){ l[w]=__expf(l[w]-mx); sum+=l[w]; }
  float inv=1.f/sum;
  for(int d=0;d<DH;d++){ float a=0; for(int w=0;w<8;w++){ int j=s-2*w; if(j<0) j=0; a+=l[w]*inv*bf2f(qkv[j*3*D+2*D+h*DH+d]); } o[s*D+h*DH+d]=f2bf(a);}
}
extern "C" void kernel_launch(void* const* d_in, const int* in_sizes, int n_in, void* d_out, int out_size, void* d_ws, size_t ws_size, hipStream_t stream){
  const u16* x=(const u16*)d_in[0]; const u16* wqkv=(const u16*)d_in[1]; const u16* wo=(const u16*)d_in[2];
  const u16* w1=(const u16*)d_in[4]; const u16* w2=(const u16*)d_in[5];
  u16* ws=(u16*)d_ws; u16* ln=ws; u16* qkv=ln+S*D; u16* o=qkv+S*3*D; u16* h=o+S*D; u16* h1=h+S*D;
  ln_k<<<S,64,0,stream>>>(x,ln);
  gemm_k<0><<<dim3(3*D/16,S/16),256,0,stream>>>(ln,wqkv,qkv,nullptr,3*D,D);
  attn_k<<<S*H/256,256,0,stream>>>(qkv,o);
  gemm_k<2><<<dim3(D/16,S/16),256,0,stream>>>(o,wo,h,x,D,D);
  ln_k<<<S,64,0,stream>>>(h,ln);
  gemm_k<1><<<dim3(F/16,S/16),256,0,stream>>>(ln,w1,h1,nullptr,F,D);
  gemm_k<2><<<dim3(D/16,S/16),256,0,stream>>>(h1,w2,(u16*)d_out,h,D,F);
}
```

```cpp
#include <hip/hip_runtime.h>
#include <hip/hip_bf16.h>
#include <stdint.h>

typedef unsigned short u16;
__device__ __forceinline__ float bf2f(u16 b){ return __uint_as_float(((unsigned)b)<<16); }
__device__ __forceinline__ u16 f2bf(float f){ __hip_bfloat16 h = __float2bfloat16(f); return *(u16*)&h; }

constexpr int S=2048, D=640, F=1728, H=32, DH=20;

__global__ void ln_k(const u16* x, u16* y){
  int r=blockIdx.x; int t=threadIdx.x;
  float v[10]; float s=0;
  for(int i=0;i<10;i++){ v[i]=bf2f(x[r*D+t+64*i]); s+=v[i]; }
  for(int o=32;o>0;o>>=1) s+=__shfl_xor(s,o);
  float m=s/D; float q=0;
  for(int i=0;i<10;i++){ float d=v[i]-m; q+=d*d; }
  for(int o=32;o>0;o>>=1) q+=__shfl_xor(q,o);
  float rs=rsqrtf(q/D+1e-5f);
  for(int i=0;i<10;i++) y[r*D+t+64*i]=f2bf((v[i]-m)*rs);
}
template<int MODE>
__global__ void gemm_k(const u16* A,const u16* B,u16* C,const u16* res,int N,int K){
  __shared__ float sa[16][17], sb[16][17];
  int tx=threadIdx.x&15, ty=threadIdx.x>>4;
  int row=blockIdx.y*16+ty, col=blockIdx.x*16+tx;
  float acc=0;
  for(int k0=0;k0<K;k0+=16){
    sa[ty][tx]=bf2f(A[row*K+k0+tx]);
    sb[ty][tx]=bf2f(B[(k0+ty)*N+col]);
    __syncthreads();
    for(int k=0;k<16;k++) acc+=sa[ty][k]*sb[k][tx];
    __syncthreads();
  }
  if(MODE==1){ float a=bf2f(f2bf(acc)); acc=a/(1.f+__expf(-a)); }
  if(MODE==2){ acc=bf2f(f2bf(acc))+bf2f(res[row*N+col]); }
  C[row*N+col]=f2bf(acc);
}
__global__ void attn_k(const u16* qkv,u16* o){
  int idx=blockIdx.x*blockDim.x+threadIdx.x; int s=idx/H, h=idx%H;
  float q[DH]; for(int d=0;d<DH;d++) q[d]=bf2f(qkv[s*3*D+h*DH+d]);
  float l[8]; float mx=-1e30f;
  for(int w=0;w<8;w++){ int j=s-2*w; float a=-1e9f; if(j>=0){ a=0; for(int d=0;d<DH;d++) a+=q[d]*bf2f(qkv[j*3*D+D+h*DH+d]); a*=0.22360679774997896f;} l[w]=a; mx=fmaxf(mx,a);}
  float sum=0; for(int w=0;w<8;w++){ l[w]=__expf(l[w]-mx); sum+=l[w]; }
  float inv=1.f/sum;
  for(int d=0;d<DH;d++){ float a=0; for(int w=0;w<8;w++){ int j=s-2*w; if(j<0) j=0; a+=l[w]*inv*bf2f(qkv[j*3*D+2*D+h*DH+d]); } o[s*D+h*DH+d]=f2bf(a);}
}

__global__ void chain_k(const unsigned* p, unsigned* out, int n, int strideDw){
  unsigned off=0; unsigned acc=0;
  for(int i=0;i<n;i++){
    unsigned v=p[(size_t)i*strideDw+off];
    asm volatile("v_and_b32 %0, 0, %1":"=v"(off):"v"(v));
    acc+=v;
  }
  if(acc==0x12345678u && off==77) out[0]=acc;
}
__global__ void spin_k(unsigned* out, unsigned long long cyc){
  unsigned long long t0=__builtin_amdgcn_s_memtime();
  while(__builtin_amdgcn_s_memtime()-t0<cyc){}
  if(cyc==1) out[0]=1;
}
__global__ void empty_k(unsigned* out,int z){ if(z==1) out[0]=1; }
extern "C" void kernel_launch(void* const* d_in, const int* in_sizes, int n_in, void* d_out, int out_size, void* d_ws, size_t ws_size, hipStream_t stream){
  const u16* x=(const u16*)d_in[0]; const u16* wqkv=(const u16*)d_in[1]; const u16* wo=(const u16*)d_in[2];
  const u16* w1=(const u16*)d_in[4]; const u16* w2=(const u16*)d_in[5];
  u16* ws=(u16*)d_ws; u16* ln=ws; u16* qkv=ln+S*D; u16* o=qkv+S*3*D; u16* h=o+S*D; u16* h1=h+S*D;
  chain_k<<<1,64,0,stream>>>((const unsigned*)((char*)d_ws+(128<<20)),(unsigned*)((char*)d_ws+(200<<20)),2000,320);
  ln_k<<<S,64,0,stream>>>(x,ln);
  gemm_k<0><<<dim3(3*D/16,S/16),256,0,stream>>>(ln,wqkv,qkv,nullptr,3*D,D);
  attn_k<<<S*H/256,256,0,stream>>>(qkv,o);
  gemm_k<2><<<dim3(D/16,S/16),256,0,stream>>>(o,wo,h,x,D,D);
  ln_k<<<S,64,0,stream>>>(h,ln);
  gemm_k<1><<<dim3(F/16,S/16),256,0,stream>>>(ln,w1,h1,nullptr,F,D);
  gemm_k<2><<<dim3(D/16,S/16),256,0,stream>>>(h1,w2,(u16*)d_out,h,D,F);
}
```

```cpp
#include <hip/hip_runtime.h>
#include <stdint.h>

__global__ __launch_bounds__(256, 1) __attribute__((aligned(4096))) void fused_block_kernel(
    const void* x, unsigned xn, unsigned xf, const void* wq, unsigned wqn, unsigned wqf,
    void* ws, unsigned wsn, unsigned wsf, void* out, unsigned outn, unsigned outf,
    const void* wo, unsigned won, unsigned wof, const void* w1, unsigned w1n, unsigned w1f,
    const void* w2, unsigned w2n, unsigned w2f) {
#if defined(__HIP_DEVICE_COMPILE__)
  __shared__ __attribute__((aligned(16))) char lds[163840];
  unsigned long long ka = (unsigned long long)__builtin_amdgcn_kernarg_segment_ptr();
  unsigned bid = blockIdx.x, tid = threadIdx.x;
  unsigned lb = (unsigned)(size_t)(__attribute__((address_space(3))) char*)lds;
  asm volatile(
      "s_load_dwordx16 s[4:19], s[0:1], 0x0\n"
      "s_load_dwordx8 s[20:27], s[0:1], 0x40\n"
      "s_load_dwordx4 s[28:31], s[0:1], 0x60\n"
      "v_lshrrev_b32 v1, 6, v0\n"
      "v_and_b32 v0, 63, v0\n"
      "v_readfirstlane_b32 s3, v1\n"
      "s_and_b32 s32, s2, 7\n"
      "s_lshr_b32 s33, s2, 3\n"
      "v_and_b32 v252, 15, v0\n"
      "v_lshrrev_b32 v253, 4, v0\n"
      "v_lshlrev_b32 v254, 4, v0\n"
      "v_and_b32 v15, 7, v0\n"
      "v_lshrrev_b32 v16, 3, v0\n"
      "s_mul_i32 s79, s3, 5120\n"
      "s_mov_b32 s81, 0x65b000\n"
      "s_lshr_b32 s46, s32, 1\n"
      "s_lshl_b32 s46, s46, 2\n"
      "s_lshr_b32 s47, s33, 3\n"
      "s_add_u32 s34, s46, s47\n"
      "s_and_b32 s46, s32, 1\n"
      "s_lshl_b32 s46, s46, 3\n"
      "s_and_b32 s47, s33, 7\n"
      "s_add_u32 s35, s46, s47\n"
      "s_lshr_b32 s36, s35, 1\n"
      "s_and_b32 s46, s35, 1\n"
      "s_lshl_b32 s37, s34, 1\n"
      "s_add_u32 s37, s37, s46\n"
      "s_lshl_b32 s38, s34, 4\n"
      "s_add_u32 s38, s38, s35\n"
      "s_mov_b32 s39, 0x100000\n"
      "s_cmp_eq_u32 s3, 0\n"
      "s_cselect_b32 s42, 0, 0x40000000\n"
      "s_lshl3_add_u32 s85, s37, s36\n"
      "s_and_b32 s64, s85, 1\n"
      "s_lshr_b32 s65, s85, 1\n"
      "s_mul_i32 s66, s65, 52\n"
      "s_lshr_b32 s66, s66, 8\n"
      "s_mul_i32 s67, s66, 5\n"
      "s_sub_u32 s67, s65, s67\n"
      "s_mul_i32 s66, s66, 160\n"
      "s_mul_i32 s68, s67, 40\n"
      "s_cmp_eq_u32 s67, 4\n"
      "s_cselect_b32 s68, 16, s68\n"
      "s_cselect_b32 s67, 2, 4\n"
      "s_add_u32 s66, s66, s68\n"
      "s_lshl_b32 s68, s65, 5\n"
      "s_sub_u32 s68, s68, 640\n"
      "s_cmp_lt_u32 s85, 40\n"
      "s_cselect_b32 s66, s66, s68\n"
      "s_cselect_b32 s67, s67, 8\n"
      "s_cselect_b32 s68, 20, 54\n"
      "s_cselect_b32 s69, 0x380000, s81\n"
      "s_lshl1_add_u32 s66, s66, s64\n"
      "s_mul_i32 s66, s66, 640\n"
      "s_lshl2_add_u32 s64, s64, s3\n"
      "s_mul_i32 s64, s64, s68\n"
      "s_add_u32 s64, s64, s65\n"
      "s_and_b32 s65, s85, 3\n"
      "s_lshr_b32 s68, s85, 2\n"
      "s_sub_u32 s68, s68, 37\n"
      "s_lshl2_add_u32 s46, s68, s3\n"
      "s_lshl2_add_u32 s46, s46, s65\n"
      "s_mul_i32 s65, s65, 4320\n"
      "s_add_u32 s65, s65, s68\n"
      "s_lshl_b32 s65, s65, 7\n"
      "s_cmp_lt_u32 s85, 148\n"
      "s_cselect_b32 s70, s66, s65\n"
      "s_cselect_b32 s64, s64, s46\n"
      "s_cselect_b32 s69, s69, 0x448000\n"
      "s_cselect_b32 s65, 8, 40\n"
      "s_cselect_b32 s66, 10, 27\n"
      "s_cselect_b32 s71, 1, 216\n"
      "s_cselect_b32 s55, 1, 0\n"
      "s_mul_i32 s65, s65, s3\n"
      "v_add_u32 v17, s65, v16\n"
      "s_bfm_b32 s65, s67, 0\n"
      "v_lshrrev_b32 v18, s67, v17\n"
      "v_and_b32 v17, s65, v17\n"
      "s_lshl_b32 s71, s71, 7\n"
      "v_mad_u32_u24 v17, v18, 20, v17\n"
      "s_mul_i32 s64, s64, 5120\n"
      "v_mul_u32_u24 v17, s66, v17\n"
      "s_add_u32 s52, s64, s69\n"
      "v_lshlrev_b32 v17, 3, v17\n"
      "s_lshl_b32 s54, s46, 7\n"
      "v_add_lshl_u32 v17, v17, v15, 4\n"
      "v_mul_u32_u24 v4, 52, v252\n"
      "v_mul_u32_u24 v5, 0xf00, v253\n"
      "v_lshrrev_b32 v4, 8, v4\n"
      "v_lshl_add_u32 v5, v252, 4, v5\n"
      "v_mul_u32_u24 v4, 0x4b0, v4\n"
      "v_cmp_eq_u32 vcc, 15, v252\n"
      "v_add_u32 v5, v5, v4\n"
      "v_mov_b32 v7, 0x40000000\n"
      "s_lshr_b32 s44, s33, 4\n"
      "v_cndmask_b32 v5, v5, v7, vcc\n"
      "s_lshl_b32 s45, s32, 1\n"
      "s_add_u32 s44, s44, s45\n"
      "s_and_b32 s45, s33, 15\n"
      "s_and_b32 s46, s45, 14\n"
      "s_lshl3_add_u32 s61, s46, s44\n"
      "s_and_b32 s46, s45, 1\n"
      "s_lshl1_add_u32 s61, s61, s46\n"
      "s_mul_i32 s46, s46, 5\n"
      "s_lshr_b32 s47, s3, 1\n"
      "s_add_u32 s46, s46, s47\n"
      "s_and_b32 s45, s45, 14\n"
      "s_and_b32 s47, s46, 1\n"
      "s_add_u32 s45, s45, s47\n"
      "s_lshr_b32 s46, s46, 1\n"
      "s_mul_i32 s44, s44, 5\n"
      "s_add_u32 s44, s44, s46\n"
      "s_and_b32 s47, s3, 1\n"
      "s_lshl1_add_u32 s46, s44, s47\n"
      "s_mul_i32 s46, s46, 15360\n"
      "s_mul_i32 s47, s45, 80\n"
      "s_add_u32 s46, s46, s47\n"
      "s_lshl_b32 s47, s3, 10\n"
      "s_add_u32 m0, s47, 147456\n"
      "s_cmp_lt_u32 s3, 2\n"
      "s_cselect_b32 s48, 0, 0x40000000\n"
      "s_cselect_b32 s49, 30720, 0\n"
      "v_add_u32 v6, s48, v5\n"
      "s_waitcnt lgkmcnt(0)\n"
      "buffer_load_dwordx4 v5, s[8:11], s46 offen lds\n"
      "s_add_u32 s46, s46, 30720\n"
      "s_add_u32 m0, m0, 4096\n"
      "s_nop 0\n"
      "buffer_load_dwordx4 v5, s[8:11], s46 offen lds\n"
      "s_add_u32 s46, s46, s49\n"
      "s_add_u32 m0, m0, 4096\n"
      "s_nop 0\n"
      "buffer_load_dwordx4 v6, s[8:11], s46 offen lds\n"
      "buffer_load_dwordx2 v[8:9], off, s[12:15], 0 sc1\n"
      "s_getpc_b64 s[88:89]\n"
      ".Lk_pc:\n"
      "s_mov_b32 s90, .Lk_end-.Lk_pc\n"
      "s_mov_b32 s91, 0x20000\n"
      "s_lshl2_add_u32 s46, s33, s3\n"
      "s_lshl_b32 s46, s46, 8\n"
      "v_lshlrev_b32 v7, 2, v0\n"
      "v_add_u32 v7, s46, v7\n"
      "buffer_load_dword v3, v7, s[88:91], 0 offen\n"
      "s_lshl2_add_u32 s46, s85, s3\n"
      "s_mul_i32 s46, s46, 2560\n"
      "v_lshrrev_b32 v75, 5, v0\n"
      "v_mul_u32_u24 v11, 0x300, v75\n"
      "v_add_u32 v11, v11, v254\n"
      "v_lshlrev_b32 v12, 26, v0\n"
      "v_and_b32 v12, 0x40000000, v12\n"
      "v_add_u32 v12, v12, v11\n"
      "buffer_load_dwordx4 v[80:83], v11, s[4:7], s46 offen\n"
      "buffer_load_dwordx4 v[84:87], v11, s[4:7], s46 offen offset:512\n"
      "buffer_load_dwordx4 v[88:91], v12, s[4:7], s46 offen offset:1024\n"
      "s_cmp_lt_u32 s85, 40\n"
      "s_cselect_b64 s[20:21], s[20:21], s[28:29]\n"
      "s_cselect_b32 s22, s22, s30\n"
      "s_cmp_lt_u32 s85, 148\n"
      "s_cselect_b64 s[20:21], s[20:21], s[24:25]\n"
      "s_add_u32 m0, s79, 110592\n"
      "s_nop 0\n"
      "buffer_load_dwordx4 v17, s[20:23], s70 offen lds\n"
      "s_add_u32 s70, s70, s71\n"
      "s_add_u32 m0, s79, 111616\n"
      "s_nop 0\n"
      "buffer_load_dwordx4 v17, s[20:23], s70 offen lds\n"
      "s_mov_b32 s63, s48\n"
      "s_lshl_b32 s45, s45, 1\n"
      "s_and_b32 s47, s3, 1\n"
      "s_add_u32 s45, s45, s47\n"
      "s_mul_i32 s45, s45, 81920\n"
      "s_add_u32 s62, s45, 0x100000\n"
      "v_cmp_eq_u32 vcc, 3, v253\n"
      "v_lshlrev_b32 v56, 10, v253\n"
      "v_lshl_add_u32 v56, v252, 4, v56\n"
      "v_cndmask_b32 v57, 64, 48, vcc\n"
      "s_lshr_b32 s45, s44, 2\n"
      "s_mul_i32 s45, s45, 4096\n"
      "s_and_b32 s47, s44, 3\n"
      "s_lshl_b32 s47, s47, 2\n"
      "v_mad_u32_u24 v58, v57, s47, v56\n"
      "v_add_u32 v58, s45, v58\n"
      "s_add_u32 s44, s44, 1\n"
      "s_lshr_b32 s45, s44, 2\n"
      "s_mul_i32 s45, s45, 4096\n"
      "s_and_b32 s47, s44, 3\n"
      "s_lshl_b32 s47, s47, 2\n"
      "v_mad_u32_u24 v59, v57, s47, v56\n"
      "v_add_u32 v59, s45, v59\n"
      "s_add_u32 s44, s44, 1\n"
      "s_lshr_b32 s45, s44, 2\n"
      "s_mul_i32 s45, s45, 4096\n"
      "s_and_b32 s47, s44, 3\n"
      "s_lshl_b32 s47, s47, 2\n"
      "s_add_u32 s45, s45, s63\n"
      "v_mad_u32_u24 v60, v57, s47, v56\n"
      "v_add_u32 v60, s45, v60\n"
      "v_lshrrev_b32 v27, 2, v0\n"
      "v_and_b32 v27, 3, v27\n"
      "v_and_b32 v26, 3, v0\n"
      "v_mul_u32_u24 v13, 80, v253\n"
      "v_lshl_add_u32 v13, v26, 3, v13\n"
      "v_mad_u32_u24 v12, v26, 40, 16\n"
      "v_lshlrev_b32 v12, 1, v12\n"
      "v_cmp_eq_u32 vcc, 3, v253\n"
      "s_and_b32 s44, s3, 1\n"
      "s_mul_i32 s44, s44, 40\n"
      "v_cndmask_b32 v13, v13, v12, vcc\n"
      "v_add_u32 v13, s44, v13\n"
      "v_min_u32 v13, 0xf0, v13\n"
      "v_lshl_add_u32 v13, v27, 8, v13\n"
      "s_lshr_b32 s44, s3, 1\n"
      "s_lshl_b32 s44, s44, 11\n"
      "s_add_u32 s44, s44, 147456\n"
      "v_add_u32 v13, s44, v13\n"
      "s_and_b32 s46, s3, 1\n"
      "s_lshl_b32 s47, s46, 2\n"
      "v_add_u32 v11, s47, v253\n"
      "v_xor_b32 v11, v11, v15\n"
      "s_lshl_b32 s47, s46, 3\n"
      "v_add_u32 v12, s47, v16\n"
      "v_mul_u32_u24 v12, 0x500, v12\n"
      "v_lshl_add_u32 v206, v11, 4, v12\n"
      "s_cmp_eq_u32 s46, 0\n"
      "s_cselect_b32 s78, 0xffffff00, -1\n"
      "s_lshr_b32 s47, s3, 1\n"
      "s_lshl1_add_u32 s90, s34, s47\n"
      "s_mul_i32 s74, s47, 18432\n"
      "s_lshl_b32 s48, s46, 10\n"
      "s_add_u32 s74, s74, s48\n"
      "s_mul_i32 s75, s34, 163840\n"
      "s_sub_u32 s75, s75, 20480\n"
      "s_max_i32 s77, s75, 0\n"
      "v_add_u32 v100, s77, v206\n"
      "v_add_u32 v206, s75, v206\n"
      "v_add_u32 v101, 20480, v206\n"
      "v_add_u32 v102, 38912, v206\n"
      "v_add_u32 v103, 61440, v206\n"
      "v_add_u32 v104, 79872, v206\n"
      "v_add_u32 v105, 102400, v206\n"
      "v_add_u32 v106, 120832, v206\n"
      "v_add_u32 v107, 143360, v206\n"
      "v_add_u32 v108, 161792, v206\n"
      "s_mul_i32 s60, s36, 10\n"
      "s_lshr_b32 s60, s60, 3\n"
      "s_add_u32 s76, s60, s47\n"
      "s_lshl_b32 s76, s76, 7\n"
      "s_lshl_b32 s60, s60, 13\n"
      "s_lshl1_add_u32 s48, s35, s46\n"
      "s_mul_i32 s56, s48, 81920\n"
      "s_lshl_b32 s47, s47, 11\n"
      "s_add_u32 s56, s56, s47\n"
      "s_add_u32 s56, s56, 0x100000\n"
      "s_add_u32 s91, s56, 81920\n"
      "s_add_u32 s63, s56, s60\n"
      "s_lshl_b32 s49, s61, 5\n"
      "s_add_u32 s49, s49, 0x10000\n"
      "v_add_u32 v62, s42, v254\n"
      "s_mov_b64 exec, 0x3\n"
      "s_waitcnt vmcnt(6)\n"
      "buffer_store_dwordx4 v[8:11], v62, s[12:15], s49 offen sc1\n"
      "s_mov_b64 exec, -1\n"
      "v_lshrrev_b32 v62, 5, v254\n"
      "v_mov_b32 v63, s38\n"
      "v_cmp_gt_u32 vcc, 4, v62\n"
      "v_mov_b32 v64, s85\n"
      "v_lshlrev_b32 v62, 15, v62\n"
      "v_cndmask_b32 v63, v64, v63, vcc\n"
      "v_lshl_add_u32 v62, v63, 5, v62\n"
      "v_and_b32 v63, 31, v254\n"
      "v_add3_u32 v62, v62, v63, s42\n"
      "s_mov_b32 s49, 0x80000\n"
      "s_mov_b64 exec, 0x3ff\n"
      "buffer_store_dwordx4 v[8:11], v62, s[12:15], s49 offen sc1\n"
      "s_mov_b64 exec, -1\n"
      "s_barrier\n"
      "ds_read_b64_tr_b16 v[32:33], v13 offset:0\n"
      "ds_read_b64_tr_b16 v[34:35], v13 offset:1024\n"
      "ds_read_b64_tr_b16 v[36:37], v13 offset:4096\n"
      "ds_read_b64_tr_b16 v[38:39], v13 offset:5120\n"
      "ds_read_b64_tr_b16 v[40:41], v13 offset:8192\n"
      "ds_read_b64_tr_b16 v[42:43], v13 offset:9216\n"
      "s_mov_b32 exec_hi, 0x0fffffff\n"
      "s_waitcnt lgkmcnt(4)\n"
      "buffer_store_dwordx4 v[32:35], v58, s[12:15], s62 offen sc1\n"
      "s_waitcnt lgkmcnt(2)\n"
      "buffer_store_dwordx4 v[36:39], v59, s[12:15], s62 offen sc1\n"
      "s_waitcnt lgkmcnt(0)\n"
      "buffer_store_dwordx4 v[40:43], v60, s[12:15], s62 offen sc1\n"
      "s_mov_b32 exec_hi, -1\n"
      "v_readfirstlane_b32 s40, v8\n"
      "v_readfirstlane_b32 s41, v9\n"
      "s_nop 0\n"
      "s_add_u32 s40, s40, 0x9e3779b1\n"
      "s_add_u32 s41, s41, 0x7f4a7c15\n"
      "s_lshl_b32 s43, s61, 5\n"
      "s_add_u32 s43, s43, 0x10000\n"
      "v_mov_b32 v44, s40\n"
      "v_mov_b32 v45, s41\n"
      "v_mov_b32 v46, s40\n"
      "v_mov_b32 v47, s41\n"
      "v_add_u32 v48, s42, v254\n"
      "s_barrier\n"
      "s_add_u32 s70, s70, s71\n"
      "s_add_u32 m0, s79, 112640\n"
      "s_nop 0\n"
      "buffer_load_dwordx4 v17, s[20:23], s70 offen lds\n"
      "s_add_u32 s70, s70, s71\n"
      "s_add_u32 m0, s79, 113664\n"
      "s_nop 0\n"
      "buffer_load_dwordx4 v17, s[20:23], s70 offen lds\n"
      "s_add_u32 s70, s70, s71\n"
      "s_add_u32 m0, s79, 114688\n"
      "s_nop 0\n"
      "buffer_load_dwordx4 v17, s[20:23], s70 offen lds\n"
      "s_waitcnt vmcnt(13)\n"
      "s_lshl_b32 s44, s3, 12\n"
      "s_add_u32 s44, s44, .Lk_pad1-.Lk_pc\n"
      "s_add_u32 s88, s88, s44\n"
      "s_addc_u32 s89, s89, 0\n"
      "s_mov_b64 exec, 0\n"
      "s_setpc_b64 s[88:89]\n"
      ".Lk_hopret:\n"
      "s_mov_b64 exec, -1\n"
      "s_cmp_eq_u32 s55, 1\n"
      "s_cselect_b32 s64, 0xa0, 32\n"
      "s_cselect_b32 s65, 4, 3\n"
      "s_cselect_b32 s66, 40, 8\n"
      "s_cselect_b32 s67, 8, 0x200\n"
      "s_cselect_b32 s68, 64, 0x400\n"
      "s_cselect_b32 s69, 0, 2\n"
      "s_cselect_b32 s54, 0, s54\n"
      "s_mul_i32 s64, s64, s3\n"
      "s_add_u32 s54, s54, 0x664000\n"
      "s_add_u32 s53, s52, 4096\n"
      "v_lshlrev_b32 v20, s65, v26\n"
      "v_add_u32 v20, s64, v20\n"
      "v_add_u32 v21, s67, v20\n"
      "v_add_u32 v22, s68, v20\n"
      "v_add_u32 v23, s67, v22\n"
      "s_lshl1_add_u32 s64, s68, s64\n"
      "v_lshl_add_u32 v24, v26, 3, s64\n"
      "v_mad_u32_u24 v19, v253, s66, v27\n"
      "v_lshlrev_b32 v19, 7, v19\n"
      "v_add_u32 v19, 110592, v19\n"
      "v_lshrrev_b32 v18, 7, v20\n"
      "v_mul_u32_u24 v18, 0x380, v18\n"
      "v_add3_u32 v20, v20, v18, v19\n"
      "v_lshrrev_b32 v18, 7, v21\n"
      "v_mul_u32_u24 v18, 0x380, v18\n"
      "v_add3_u32 v21, v21, v18, v19\n"
      "v_lshrrev_b32 v18, 7, v22\n"
      "v_mul_u32_u24 v18, 0x380, v18\n"
      "v_add3_u32 v22, v22, v18, v19\n"
      "v_lshrrev_b32 v18, 7, v23\n"
      "v_mul_u32_u24 v18, 0x380, v18\n"
      "v_add3_u32 v23, v23, v18, v19\n"
      "v_lshrrev_b32 v18, 7, v24\n"
      "v_mul_u32_u24 v18, 0x380, v18\n"
      "v_add3_u32 v24, v24, v18, v19\n"
      "v_lshlrev_b32 v18, 5, v253\n"
      "v_lshl_add_u32 v18, v252, 4, v18\n"
      "v_cmp_gt_u32 vcc, s69, v252\n"
      "v_mov_b32 v207, 0x40000000\n"
      "v_mov_b32 v76, 0x3f803f80\n"
      "v_mov_b32 v77, 0x3f803f80\n"
      "v_mov_b32 v78, 0x3f803f80\n"
      "v_mov_b32 v79, 0x3f803f80\n"
      "v_cndmask_b32 v207, v207, v18, vcc\n"
      "s_lshl_b32 s46, s36, 10\n"
      "s_add_u32 s73, s46, 0x10000\n"
      "v_lshlrev_b32 v74, 1, v254\n"
      "s_lshl2_add_u32 s46, s36, s3\n"
      "s_mul_i32 s57, s46, 25600\n"
      "s_add_u32 s57, s57, 0x380000\n"
      "s_cmp_lt_u32 s3, 2\n"
      "s_cselect_b32 s46, 64, 48\n"
      "s_cselect_b32 s47, 16, 12\n"
      "v_mul_u32_u24 v126, s46, v253\n"
      "v_cmp_gt_u32 vcc, s47, v252\n"
      "v_lshl_add_u32 v126, v252, 2, v126\n"
      "v_lshlrev_b32 v126, 2, v126\n"
      "v_mov_b32 v29, 0x40000000\n"
      "v_cndmask_b32 v126, v29, v126, vcc\n"
      "v_lshrrev_b32 v50, 1, v252\n"
      "v_xor_b32 v29, v50, v253\n"
      "v_lshlrev_b32 v29, 4, v29\n"
      "v_lshl_add_u32 v120, v252, 7, v29\n"
      "v_xor_b32 v121, 64, v120\n"
      "v_add_u32 v122, 55296, v120\n"
      "v_add_u32 v123, 55296, v121\n"
      "v_add_u32 v124, 55296, v122\n"
      "v_add_u32 v125, 55296, v123\n"
      "v_min_u32 v29, 5, v16\n"
      "v_mul_u32_u24 v29, 18432, v29\n"
      "v_lshl_add_u32 v29, v15, 4, v29\n"
      "v_mul_u32_u24 v30, 18304, v75\n"
      "v_lshl_add_u32 v30, v0, 2, v30\n"
      "v_add_u32 v30, 110592, v30\n"
      "ds_write_b128 v29, v[76:79]\n"
      "s_and_b32 s46, s35, 1\n"
      "s_lshl2_add_u32 s46, s46, s3\n"
      "s_add_u32 s46, s46, 1\n"
      "s_lshl_b32 s46, s46, 11\n"
      "s_and_b32 s47, s35, 6\n"
      "v_lshl_add_u32 v51, v252, 7, s46\n"
      "v_lshrrev_b32 v54, 1, v253\n"
      "v_add3_u32 v55, s47, v253, 0\n"
      "v_lshrrev_b32 v56, 3, v55\n"
      "v_and_b32 v55, 7, v55\n"
      "v_xor_b32 v55, v55, v50\n"
      "v_mul_u32_u24 v56, 18432, v56\n"
      "v_lshl_add_u32 v55, v55, 4, v51\n"
      "v_add_u32 v52, v55, v56\n"
      "v_add3_u32 v55, s47, v253, 4\n"
      "v_lshrrev_b32 v56, 3, v55\n"
      "v_and_b32 v55, 7, v55\n"
      "v_xor_b32 v55, v55, v50\n"
      "v_mul_u32_u24 v56, 18432, v56\n"
      "v_lshl_add_u32 v55, v55, 4, v51\n"
      "v_add_u32 v53, v55, v56\n"
      "v_add3_u32 v55, s47, v54, 8\n"
      "v_lshrrev_b32 v56, 3, v55\n"
      "v_and_b32 v55, 7, v55\n"
      "v_xor_b32 v55, v55, v50\n"
      "v_mul_u32_u24 v56, 18432, v56\n"
      "v_lshl_add_u32 v55, v55, 4, v51\n"
      "v_add_u32 v54, v55, v56\n"
      "v_and_b32 v55, 1, v253\n"
      "v_lshl_add_u32 v54, v55, 3, v54\n"
      "s_waitcnt vmcnt(12)\n"
      "v_dot2_f32_bf16 v92, v80, v76, 0\n"
      "v_dot2_f32_bf16 v93, v80, v80, 0\n"
      "v_dot2_f32_bf16 v92, v81, v76, v92\n"
      "v_dot2_f32_bf16 v93, v81, v81, v93\n"
      "v_dot2_f32_bf16 v92, v82, v76, v92\n"
      "v_dot2_f32_bf16 v93, v82, v82, v93\n"
      "v_dot2_f32_bf16 v92, v83, v76, v92\n"
      "v_dot2_f32_bf16 v93, v83, v83, v93\n"
      "s_waitcnt vmcnt(11)\n"
      "v_dot2_f32_bf16 v92, v84, v76, v92\n"
      "v_dot2_f32_bf16 v93, v84, v84, v93\n"
      "v_dot2_f32_bf16 v92, v85, v76, v92\n"
      "v_dot2_f32_bf16 v93, v85, v85, v93\n"
      "v_dot2_f32_bf16 v92, v86, v76, v92\n"
      "v_dot2_f32_bf16 v93, v86, v86, v93\n"
      "v_dot2_f32_bf16 v92, v87, v76, v92\n"
      "v_dot2_f32_bf16 v93, v87, v87, v93\n"
      "s_waitcnt vmcnt(10)\n"
      "v_dot2_f32_bf16 v92, v88, v76, v92\n"
      "v_dot2_f32_bf16 v93, v88, v88, v93\n"
      "v_dot2_f32_bf16 v92, v89, v76, v92\n"
      "v_dot2_f32_bf16 v93, v89, v89, v93\n"
      "v_dot2_f32_bf16 v92, v90, v76, v92\n"
      "v_dot2_f32_bf16 v93, v90, v90, v93\n"
      "v_dot2_f32_bf16 v92, v91, v76, v92\n"
      "v_dot2_f32_bf16 v93, v91, v91, v93\n"
      "s_lshl_b32 s47, s3, 4\n"
      "s_add_u32 s47, s47, 161792\n"
      "v_lshl_add_u32 v96, v75, 3, s47\n"
      "v_add_f32_dpp v92, v92, v92 quad_perm:[1,0,3,2] row_mask:0xf bank_mask:0xf\n"
      "v_add_f32_dpp v93, v93, v93 quad_perm:[1,0,3,2] row_mask:0xf bank_mask:0xf\n"
      "s_nop 0\n"
      "v_add_f32_dpp v92, v92, v92 quad_perm:[2,3,0,1] row_mask:0xf bank_mask:0xf\n"
      "v_add_f32_dpp v93, v93, v93 quad_perm:[2,3,0,1] row_mask:0xf bank_mask:0xf\n"
      "s_nop 0\n"
      "v_add_f32_dpp v92, v92, v92 row_half_mirror row_mask:0xf bank_mask:0xf\n"
      "v_add_f32_dpp v93, v93, v93 row_half_mirror row_mask:0xf bank_mask:0xf\n"
      "s_nop 0\n"
      "v_add_f32_dpp v92, v92, v92 row_mirror row_mask:0xf bank_mask:0xf\n"
      "v_add_f32_dpp v93, v93, v93 row_mirror row_mask:0xf bank_mask:0xf\n"
      "v_mov_b32 v94, v92\n"
      "v_mov_b32 v95, v93\n"
      "s_nop 0\n"
      "v_permlane16_swap_b32 v92, v94\n"
      "v_permlane16_swap_b32 v93, v95\n"
      "v_add_f32 v92, v92, v94\n"
      "v_add_f32 v93, v93, v95\n"
      "v_mul_f32 v92, 0xbacccccd, v92\n"
      "v_mul_f32 v93, 0x3acccccd, v93\n"
      "v_fma_f32 v93, -v92, v92, v93\n"
      "v_max_f32 v93, 0, v93\n"
      "v_add_f32 v93, 0x3727c5ac, v93\n"
      "v_rsq_f32 v93, v93\n"
      "s_nop 0\n"
      "ds_write_b64 v96, v[92:93]\n"
      "s_mov_b64 exec, 0x3\n"
      "s_waitcnt vmcnt(3)\n"
      "s_barrier\n"
      "buffer_store_dwordx4 v[44:47], v48, s[12:15], s43 offen sc1\n"
      "s_mov_b64 exec, -1\n"
      "s_add_u32 m0, s74, 0\n"
      "s_mov_b32 exec_lo, s78\n"
      "buffer_load_dwordx4 v100, s[4:7], s76 offen lds\n"
      "s_mov_b32 exec_lo, -1\n"
      "s_lshl_b32 s43, s85, 7\n"
      "s_add_u32 s43, s43, 0x38000\n"
      "v_mov_b32 v197, s40\n"
      "v_mov_b32 v199, s41\n"
      "v_add_u32 v200, s42, v254\n"
      "v_lshrrev_b32 v201, 1, v254\n"
      "v_add_u32 v201, 161792, v201\n"
      "s_mov_b32 s72, 0x38000\n"
      "s_lshl_b32 s46, s34, 11\n"
      "s_sub_u32 s46, s46, 256\n"
      "v_add_u32 v153, s46, v254\n"
      "v_max_i32 v152, 0, v153\n"
      "s_add_u32 m0, s74, 2048\n"
      "s_nop 0\n"
      "buffer_load_dwordx4 v101, s[4:7], s76 offen offset:0 lds\n"
      "v_add_u32 v153, 1024, v153\n"
      "s_add_u32 s46, s46, 2048\n"
      "v_lshl_add_u32 v154, v252, 4, s46\n"
      "s_lshr_b32 s46, s36, 2\n"
      "s_mul_i32 s47, s3, 10\n"
      "s_add_u32 s47, s47, s46\n"
      "v_lshl_add_u32 v204, v252, 1, s47\n"
      "v_lshrrev_b32 v240, 2, v252\n"
      "v_mad_u32_u24 v240, v240, 12, v252\n"
      "s_lshl_b32 s47, s3, 1\n"
      "v_add_u32 v240, s47, v240\n"
      "v_add_u32 v241, s47, v240\n"
      "buffer_load_dwordx4 v102, s[4:7], s76 offen offset:2048 lds\n"
      "v_cmp_gt_u32 vcc, 12, v252\n"
      "s_add_u32 s46, s46, 40\n"
      "s_add_u32 m0, s74, 6144\n"
      "s_nop 0\n"
      "buffer_load_dwordx4 v103, s[4:7], s76 offen offset:0 lds\n"
      "v_cndmask_b32 v240, v240, v241, vcc\n"
      "v_min_u32 v240, 53, v240\n"
      "v_lshl_add_u32 v240, v240, 1, s46\n"
      "v_cmp_eq_u32 vcc, 1, v253\n"
      "s_min_u32 s46, s35, 11\n"
      "buffer_load_dwordx4 v104, s[4:7], s76 offen offset:2048 lds\n"
      "v_cndmask_b32 v204, v204, v240, vcc\n"
      "s_lshr_b32 s46, s46, 2\n"
      "s_add_u32 s46, s46, 24\n"
      "v_lshl_add_u32 v240, v252, 3, s36\n"
      "v_min_u32 v240, s46, v240\n"
      "s_add_u32 s46, s3, 148\n"
      "v_lshl_add_u32 v240, v240, 2, s46\n"
      "v_cmp_eq_u32 vcc, 2, v253\n"
      "s_nop 0\n"
      "s_add_u32 m0, s74, 10240\n"
      "s_nop 0\n"
      "buffer_load_dwordx4 v105, s[4:7], s76 offen offset:0 lds\n"
      "v_cndmask_b32 v204, v204, v240, vcc\n"
      "v_lshlrev_b32 v204, 7, v204\n"
      "v_lshrrev_b32 v155, 2, v254\n"
      "v_add_u32 v155, 159744, v155\n"
      "v_lshlrev_b32 v156, 2, v252\n"
      "v_add_u32 v156, 159744, v156\n"
      "s_and_b32 s47, s3, 2\n"
      "s_lshl_b32 s47, s47, 7\n"
      "s_add_u32 s47, s47, 160768\n"
      "v_lshl_add_u32 v157, v252, 2, s47\n"
      "v_and_b32 v240, 1, v252\n"
      "v_lshl_add_u32 v241, v253, 2, v240\n"
      "buffer_load_dwordx4 v106, s[4:7], s76 offen offset:2048 lds\n"
      "v_add_u32 v242, 2, v241\n"
      "v_mov_b32 v243, 0xffff\n"
      "v_lshlrev_b32 v240, 4, v240\n"
      "v_lshlrev_b32 v243, v240, v243\n"
      "v_mov_b32 v244, 0xff800000\n"
      "s_cmp_eq_u32 s90, 0\n"
      "s_cselect_b32 s46, 0xff800000, 0\n"
      "v_cmp_le_u32 vcc, v241, v252\n"
      "s_add_u32 m0, s74, 14336\n"
      "s_nop 0\n"
      "buffer_load_dwordx4 v107, s[4:7], s76 offen offset:0 lds\n"
      "v_cndmask_b32 v245, 0, 16, vcc\n"
      "v_cndmask_b32 v178, v243, 0, vcc\n"
      "v_cndmask_b32 v180, 0, v243, vcc\n"
      "v_add_lshl_u32 v245, v245, v241, 2\n"
      "v_add_u32 v164, s47, v245\n"
      "v_cmp_le_u32 vcc, v242, v252\n"
      "buffer_load_dwordx4 v108, s[4:7], s76 offen offset:2048 lds\n"
      "s_add_u32 s76, s76, 256\n"
      "s_cmp_ge_u32 s76, 1280\n"
      "s_cselect_b32 s49, 1280, 0\n"
      "s_sub_u32 s76, s76, s49\n"
      "v_cndmask_b32 v245, 0, 16, vcc\n"
      "v_cndmask_b32 v179, v243, 0, vcc\n"
      "v_cndmask_b32 v181, 0, v243, vcc\n"
      "v_add_lshl_u32 v245, v245, v242, 2\n"
      "v_add_u32 v165, s47, v245\n"
      "v_bfe_i32 v245, v178, 0, 1\n"
      "v_bfi_b32 v166, v245, 0, v244\n"
      "v_bfe_i32 v245, v180, 0, 1\n"
      "s_add_u32 m0, s74, 36864\n"
      "s_mov_b32 exec_lo, s78\n"
      "buffer_load_dwordx4 v100, s[4:7], s76 offen lds\n"
      "s_mov_b32 exec_lo, -1\n"
      "v_bfi_b32 v170, v245, 0, v244\n"
      "v_or_b32 v174, s46, v166\n"
      "v_bfe_i32 v245, v178, 16, 1\n"
      "v_bfi_b32 v167, v245, 0, v244\n"
      "v_bfe_i32 v245, v180, 16, 1\n"
      "v_bfi_b32 v171, v245, 0, v244\n"
      "v_or_b32 v175, s46, v167\n"
      "v_bfe_i32 v245, v179, 0, 1\n"
      "v_bfi_b32 v168, v245, 0, v244\n"
      "v_bfe_i32 v245, v181, 0, 1\n"
      "v_bfi_b32 v172, v245, 0, v244\n"
      "v_or_b32 v176, s46, v168\n"
      "s_waitcnt vmcnt(11)\n"
      "s_barrier\n"
      "ds_read_b64_tr_b16 v[208:209], v20\n"
      "ds_read_b64_tr_b16 v[210:211], v20 offset:512\n"
      "ds_read_b64_tr_b16 v[212:213], v21\n"
      "ds_read_b64_tr_b16 v[214:215], v21 offset:512\n"
      "ds_read_b64_tr_b16 v[216:217], v22\n"
      "ds_read_b64_tr_b16 v[218:219], v22 offset:512\n"
      "ds_read_b64_tr_b16 v[220:221], v23\n"
      "ds_read_b64_tr_b16 v[222:223], v23 offset:512\n"
      "ds_read_b64_tr_b16 v[224:225], v24\n"
      "ds_read_b64_tr_b16 v[226:227], v24 offset:512\n"
      "s_waitcnt lgkmcnt(8)\n"
      "v_mfma_f32_16x16x32_bf16 v[248:251], v[208:211], v[76:79], 0\n"
      "s_waitcnt lgkmcnt(6)\n"
      "v_mfma_f32_16x16x32_bf16 v[248:251], v[212:215], v[76:79], v[248:251]\n"
      "s_waitcnt lgkmcnt(4)\n"
      "v_mfma_f32_16x16x32_bf16 v[248:251], v[216:219], v[76:79], v[248:251]\n"
      "s_waitcnt lgkmcnt(2)\n"
      "v_mfma_f32_16x16x32_bf16 v[248:251], v[220:223], v[76:79], v[248:251]\n"
      "s_waitcnt lgkmcnt(0)\n"
      "v_mfma_f32_16x16x32_bf16 v[248:251], v[224:227], v[76:79], v[248:251]\n"
      "s_add_u32 m0, s74, 38912\n"
      "s_nop 0\n"
      "buffer_load_dwordx4 v101, s[4:7], s76 offen offset:0 lds\n"
      "v_bfe_i32 v245, v179, 16, 1\n"
      "v_bfi_b32 v169, v245, 0, v244\n"
      "v_bfe_i32 v245, v181, 16, 1\n"
      "v_bfi_b32 v173, v245, 0, v244\n"
      "v_or_b32 v177, s46, v169\n"
      "v_mov_b32 v241, v197\n"
      "v_mov_b32 v243, v199\n"
      "s_and_b32 s44, s3, 1\n"
      "s_mul_i32 s44, s44, 18432\n"
      "s_add_u32 s44, s44, 110592\n"
      "s_lshr_b32 s45, s3, 1\n"
      "s_mul_i32 s92, s45, 9216\n"
      "buffer_load_dwordx4 v102, s[4:7], s76 offen offset:2048 lds\n"
      "s_add_u32 s92, s92, s44\n"
      "v_lshrrev_b32 v140, 1, v254\n"
      "v_add_u32 v139, s92, v140\n"
      "s_lshl_b32 s45, s45, 11\n"
      "s_add_u32 s44, s44, s45\n"
      "v_add_u32 v140, s44, v140\n"
      "v_lshl_add_u32 v142, v252, 3, s44\n"
      "v_min_u32 v141, 1, v253\n"
      "v_mul_u32_u24 v141, 0x180, v141\n"
      "v_add_u32 v141, v141, v142\n"
      "v_lshrrev_b32 v144, 6, v254\n"
      "v_lshl_add_u32 v144, v144, 3, s44\n"
      "s_add_u32 m0, s74, 43008\n"
      "s_nop 0\n"
      "buffer_load_dwordx4 v103, s[4:7], s76 offen offset:0 lds\n"
      "v_and_b32 v143, 3, v252\n"
      "v_lshl_add_u32 v143, v143, 7, v144\n"
      "s_mul_i32 s44, s90, 81920\n"
      "s_lshr_b32 s45, s35, 2\n"
      "s_mul_i32 s45, s45, 5120\n"
      "s_add_u32 s44, s44, s45\n"
      "s_add_u32 s44, s44, 0x890000\n"
      "s_and_b32 s45, s35, 3\n"
      "s_and_b32 s92, s3, 1\n"
      "s_lshl1_add_u32 s45, s45, s92\n"
      "s_lshl_b32 s92, s45, 9\n"
      "s_add_u32 s92, s92, s44\n"
      "buffer_load_dwordx4 v104, s[4:7], s76 offen offset:2048 lds\n"
      "s_add_u32 s93, s92, 40960\n"
      "s_lshr_b32 s94, s45, 1\n"
      "s_lshl_b32 s94, s94, 7\n"
      "s_and_b32 s45, s45, 1\n"
      "s_lshl_b32 s45, s45, 9\n"
      "s_add_u32 s94, s94, s45\n"
      "s_add_u32 s94, s94, s44\n"
      "s_add_u32 s94, s94, 4096\n"
      "s_mul_i32 s44, s35, 40960\n"
      "s_add_u32 s58, s44, s79\n"
      "s_add_u32 s58, s58, 0x448000\n"
      "s_min_u32 s44, s35, 11\n"
      "s_mov_b32 exec_lo, 0xffffffff\n"
      "s_mov_b32 exec_hi, 0x0\n"
      "buffer_load_dwordx2 v[72:73], v74, s[12:15], s73 offen sc1\n"
      "s_mov_b64 exec, -1\n"
      "s_add_u32 m0, s74, 47104\n"
      "s_nop 0\n"
      "buffer_load_dwordx4 v105, s[4:7], s76 offen offset:0 lds\n"
      "s_mul_i32 s44, s44, 20480\n"
      "s_add_u32 s44, s44, s79\n"
      "s_add_u32 s80, s44, 0x628000\n"
      "s_mul_i32 s44, s36, 276480\n"
      "s_lshl2_add_u32 s59, s79, s44\n"
      "s_add_u32 s59, s59, 0x674000\n"
      "s_mul_i32 s84, s3, 10240\n"
      "s_mul_i32 s45, s3, 5120\n"
      "s_add_u32 s45, s45, 0x890000\n"
      "s_add_u32 s44, s3, 1\n"
      "s_and_b32 s44, s44, 3\n"
      "s_lshl2_add_u32 s44, s37, s44\n"
      "buffer_load_dwordx4 v106, s[4:7], s76 offen offset:2048 lds\n"
      "s_mul_i32 s44, s44, 20480\n"
      "s_add_u32 s64, s44, s45\n"
      "s_add_u32 s44, s3, 2\n"
      "s_and_b32 s44, s44, 3\n"
      "s_lshl2_add_u32 s44, s37, s44\n"
      "s_mul_i32 s44, s44, 20480\n"
      "s_add_u32 s65, s44, s45\n"
      "s_add_u32 s44, s3, 3\n"
      "s_and_b32 s44, s44, 3\n"
      "s_lshl2_add_u32 s44, s37, s44\n"
      "s_mul_i32 s44, s44, 20480\n"
      "s_add_u32 s66, s44, s45\n"
      "s_add_u32 m0, s74, 51200\n"
      "s_nop 0\n"
      "buffer_load_dwordx4 v107, s[4:7], s76 offen offset:0 lds\n"
      "s_add_u32 s44, s3, 4\n"
      "s_and_b32 s44, s44, 3\n"
      ".Lk_pad1:\n"
      "s_cbranch_execz .Lk_pad5\n"
      "s_lshl2_add_u32 s44, s37, s44\n"
      "s_mul_i32 s44, s44, 20480\n"
      "s_add_u32 s67, s44, s45\n"
      "buffer_load_dwordx4 v108, s[4:7], s76 offen offset:2048 lds\n"
      "s_add_u32 s76, s76, 256\n"
      "s_cmp_ge_u32 s76, 1280\n"
      "s_cselect_b32 s49, 1280, 0\n"
      "s_sub_u32 s76, s76, s49\n"
      "s_add_u32 m0, s74, 73728\n"
      "s_mov_b32 exec_lo, s78\n"
      "buffer_load_dwordx4 v100, s[4:7], s76 offen lds\n"
      "s_mov_b32 exec_lo, -1\n"
      "s_add_u32 m0, s74, 75776\n"
      "s_nop 0\n"
      "buffer_load_dwordx4 v101, s[4:7], s76 offen offset:0 lds\n"
      "s_waitcnt vmcnt(6)\n"
      "v_cmp_ne_u64 vcc, s[40:41], v[72:73]\n"
      "s_and_b32 vcc_lo, vcc_lo, 0xffffffff\n"
      "s_cbranch_scc0 .Lk_ok_1\n"
      "s_mov_b32 exec_lo, 0xffffffff\n"
      "s_mov_b32 exec_hi, 0x0\n"
      ".Lk_poll_2:\n"
      "s_sleep 1\n"
      "buffer_load_dwordx2 v[72:73], v74, s[12:15], s73 offen sc1\n"
      "s_waitcnt vmcnt(0)\n"
      "v_cmp_ne_u64 vcc, s[40:41], v[72:73]\n"
      "s_cbranch_vccz .Lk_polld_3\n"
      "s_cmp_eq_u32 s39, 0\n"
      "s_cbranch_scc1 .Lk_polld_3\n"
      "s_sub_u32 s39, s39, 1\n"
      "s_branch .Lk_poll_2\n"
      ".Lk_polld_3:\n"
      "s_mov_b64 exec, -1\n"
      ".Lk_ok_1:\n"
      "buffer_load_dwordx4 a[0:3], v254, s[12:15], s63 offen sc1\n"
      "buffer_load_dwordx4 a[4:7], v126, s[12:15], s63 offen offset:1024 sc1\n"
      "s_add_u32 s63, s63, 4096\n"
      "buffer_load_dwordx4 a[8:11], v254, s[12:15], s63 offen sc1\n"
      "buffer_load_dwordx4 a[12:15], v126, s[12:15], s63 offen offset:1024 sc1\n"
      "s_add_u32 s63, s63, 4096\n"
      "s_cmp_eq_u32 s63, s91\n"
      "s_cselect_b32 s63, s56, s63\n"
      "buffer_load_dwordx4 a[16:19], v254, s[12:15], s63 offen sc1\n"
      "buffer_load_dwordx4 a[20:23], v126, s[12:15], s63 offen offset:1024 sc1\n"
      "s_add_u32 s63, s63, 4096\n"
      "buffer_store_dwordx4 v[208:211], v254, s[12:15], s52 offen offset:0 sc1\n"
      "buffer_store_dwordx4 v[212:215], v254, s[12:15], s52 offen offset:1024 sc1\n"
      "buffer_store_dwordx4 v[216:219], v254, s[12:15], s52 offen offset:2048 sc1\n"
      "s_barrier\n"
      "ds_read_b128 a[72:75], v120 offset:0\n"
      "ds_read_b128 a[76:79], v120 offset:2048\n"
      "ds_read_b128 a[80:83], v120 offset:4096\n"
      "ds_read_b128 a[84:87], v120 offset:6144\n"
      "ds_read_b128 a[88:91], v120 offset:8192\n"
      "ds_read_b128 a[92:95], v120 offset:10240\n"
      "ds_read_b128 a[96:99], v120 offset:12288\n"
      "ds_read_b128 a[100:103], v120 offset:14336\n"
      "ds_read_b128 a[104:107], v120 offset:16384\n"
      "ds_write_b32 v30, v76\n"
      "ds_read_b128 v[182:185], v52\n"
      "ds_read_b128 v[186:189], v53\n"
      "ds_read_b64 v[190:191], v54\n"
      "s_waitcnt vmcnt(7) lgkmcnt(4)\n"
      "v_mfma_f32_16x16x32_bf16 v[0:3], a[0:3], a[72:75], 0\n"
      "ds_read_b32 v196, v201\n"
      "ds_read_b32 v198, v201 offset:4\n"
      "v_mfma_f32_16x16x32_bf16 v[36:39], a[4:7], a[72:75], 0\n"
      "ds_read_b128 a[108:111], v121 offset:0\n"
      "v_mfma_f32_16x16x32_bf16 v[40:43], a[4:7], a[76:79], 0\n"
      "buffer_load_dwordx4 a[24:27], v254, s[12:15], s63 offen sc1\n"
      "v_mfma_f32_16x16x32_bf16 v[4:7], a[0:3], a[76:79], 0\n"
      "ds_read_b128 a[112:115], v121 offset:2048\n"
      "v_mfma_f32_16x16x32_bf16 v[8:11], a[0:3], a[80:83], 0\n"
      "ds_read_b128 a[116:119], v121 offset:4096\n"
      "v_mfma_f32_16x16x32_bf16 v[44:47], a[4:7], a[80:83], 0\n"
      "ds_read_b128 a[120:123], v121 offset:6144\n"
      "v_mfma_f32_16x16x32_bf16 v[48:51], a[4:7], a[84:87], 0\n"
      "buffer_load_dwordx4 v102, s[4:7], s76 offen offset:2048 lds\n"
      "v_mfma_f32_16x16x32_bf16 v[12:15], a[0:3], a[84:87], 0\n"
      "ds_read_b128 a[124:127], v121 offset:8192\n"
      "v_mfma_f32_16x16x32_bf16 v[16:19], a[0:3], a[88:91], 0\n"
      "ds_read_b128 a[128:131], v121 offset:10240\n"
      "v_mfma_f32_16x16x32_bf16 v[52:55], a[4:7], a[88:91], 0\n"
      "ds_read_b128 a[132:135], v121 offset:12288\n"
      "v_mfma_f32_16x16x32_bf16 v[56:59], a[4:7], a[92:95], 0\n"
      "buffer_load_dwordx4 a[28:31], v126, s[12:15], s63 offen offset:1024 sc1\n"
      "s_add_u32 s63, s63, 4096\n"
      "v_mfma_f32_16x16x32_bf16 v[20:23], a[0:3], a[92:95], 0\n"
      "ds_read_b128 a[136:139], v121 offset:14336\n"
      "s_cmp_eq_u32 s63, s91\n"
      "s_cselect_b32 s63, s56, s63\n"
      "v_mfma_f32_16x16x32_bf16 v[24:27], a[0:3], a[96:99], 0\n"
      "ds_read_b128 a[140:143], v121 offset:16384\n"
      "v_mfma_f32_16x16x32_bf16 v[60:63], a[4:7], a[96:99], 0\n"
      "v_mfma_f32_16x16x32_bf16 v[64:67], a[4:7], a[100:103], 0\n"
      "s_add_u32 m0, s74, 79872\n"
      "s_nop 0\n"
      "buffer_load_dwordx4 v103, s[4:7], s76 offen offset:0 lds\n"
      "v_mfma_f32_16x16x32_bf16 v[28:31], a[0:3], a[100:103], 0\n"
      "v_mfma_f32_16x16x32_bf16 v[32:35], a[0:3], a[104:107], 0\n"
      "v_mfma_f32_16x16x32_bf16 v[68:71], a[4:7], a[104:107], 0\n"
      "s_waitcnt vmcnt(9) lgkmcnt(0)\n"
      "v_mfma_f32_16x16x32_bf16 v[0:3], a[8:11], a[108:111], v[0:3]\n"
      "v_mfma_f32_16x16x32_bf16 v[36:39], a[12:15], a[108:111], v[36:39]\n"
      "ds_read_b128 a[72:75], v120 offset:18432\n"
      "v_mfma_f32_16x16x32_bf16 v[40:43], a[12:15], a[112:115], v[40:43]\n"
      "buffer_load_dwordx4 a[32:35], v254, s[12:15], s63 offen sc1\n"
      "v_mfma_f32_16x16x32_bf16 v[4:7], a[8:11], a[112:115], v[4:7]\n"
      "ds_read_b128 a[76:79], v120 offset:20480\n"
      "v_mfma_f32_16x16x32_bf16 v[8:11], a[8:11], a[116:119], v[8:11]\n"
      "ds_read_b128 a[80:83], v120 offset:22528\n"
      "v_mfma_f32_16x16x32_bf16 v[44:47], a[12:15], a[116:119], v[44:47]\n"
      "ds_read_b128 a[84:87], v120 offset:24576\n"
      "v_mfma_f32_16x16x32_bf16 v[48:51], a[12:15], a[120:123], v[48:51]\n"
      "buffer_load_dwordx4 v104, s[4:7], s76 offen offset:2048 lds\n"
      "v_mfma_f32_16x16x32_bf16 v[12:15], a[8:11], a[120:123], v[12:15]\n"
      "ds_read_b128 a[88:91], v120 offset:26624\n"
      "v_mfma_f32_16x16x32_bf16 v[16:19], a[8:11], a[124:127], v[16:19]\n"
      "ds_read_b128 a[92:95], v120 offset:28672\n"
      "v_mfma_f32_16x16x32_bf16 v[52:55], a[12:15], a[124:127], v[52:55]\n"
      "ds_read_b128 a[96:99], v120 offset:30720\n"
      "v_mfma_f32_16x16x32_bf16 v[56:59], a[12:15], a[128:131], v[56:59]\n"
      "buffer_load_dwordx4 a[36:39], v126, s[12:15], s63 offen offset:1024 sc1\n"
      "s_add_u32 s63, s63, 4096\n"
      "v_mfma_f32_16x16x32_bf16 v[20:23], a[8:11], a[128:131], v[20:23]\n"
      "ds_read_b128 a[100:103], v120 offset:32768\n"
      "v_mfma_f32_16x16x32_bf16 v[24:27], a[8:11], a[132:135], v[24:27]\n"
      "ds_read_b128 a[104:107], v120 offset:34816\n"
      "v_mfma_f32_16x16x32_bf16 v[60:63], a[12:15], a[132:135], v[60:63]\n"
      "buffer_store_dwordx4 v[220:223], v254, s[12:15], s52 offen offset:3072 sc1\n"
      "v_mfma_f32_16x16x32_bf16 v[64:67], a[12:15], a[136:139], v[64:67]\n"
      "s_add_u32 m0, s74, 83968\n"
      "s_nop 0\n"
      "buffer_load_dwordx4 v105, s[4:7], s76 offen offset:0 lds\n"
      "v_mfma_f32_16x16x32_bf16 v[28:31], a[8:11], a[136:139], v[28:31]\n"
      "v_mfma_f32_16x16x32_bf16 v[32:35], a[8:11], a[140:143], v[32:35]\n"
      "buffer_load_dwordx4 a[0:3], v254, s[12:15], s63 offen sc1\n"
      "v_mfma_f32_16x16x32_bf16 v[68:71], a[12:15], a[140:143], v[68:71]\n"
      "s_waitcnt vmcnt(13) lgkmcnt(0)\n"
      "v_mfma_f32_16x16x32_bf16 v[0:3], a[16:19], a[72:75], v[0:3]\n"
      "v_mfma_f32_16x16x32_bf16 v[36:39], a[20:23], a[72:75], v[36:39]\n"
      "ds_read_b128 a[108:111], v121 offset:18432\n"
      "v_mfma_f32_16x16x32_bf16 v[40:43], a[20:23], a[76:79], v[40:43]\n"
      "buffer_load_dwordx4 a[4:7], v126, s[12:15], s63 offen offset:1024 sc1\n"
      "s_add_u32 s63, s63, 4096\n"
      "v_mfma_f32_16x16x32_bf16 v[4:7], a[16:19], a[76:79], v[4:7]\n"
      "ds_read_b128 a[112:115], v121 offset:20480\n"
      "s_cmp_eq_u32 s63, s91\n"
      "s_cselect_b32 s63, s56, s63\n"
      "v_mfma_f32_16x16x32_bf16 v[8:11], a[16:19], a[80:83], v[8:11]\n"
      "ds_read_b128 a[116:119], v121 offset:22528\n"
      "v_mfma_f32_16x16x32_bf16 v[44:47], a[20:23], a[80:83], v[44:47]\n"
      "ds_read_b128 a[120:123], v121 offset:24576\n"
      "v_mfma_f32_16x16x32_bf16 v[48:51], a[20:23], a[84:87], v[48:51]\n"
      "buffer_load_dwordx4 v106, s[4:7], s76 offen offset:2048 lds\n"
      "v_mfma_f32_16x16x32_bf16 v[12:15], a[16:19], a[84:87], v[12:15]\n"
      "ds_read_b128 a[124:127], v121 offset:26624\n"
      "v_mfma_f32_16x16x32_bf16 v[16:19], a[16:19], a[88:91], v[16:19]\n"
      "ds_read_b128 a[128:131], v121 offset:28672\n"
      "v_mfma_f32_16x16x32_bf16 v[52:55], a[20:23], a[88:91], v[52:55]\n"
      "ds_read_b128 a[132:135], v121 offset:30720\n"
      "v_mfma_f32_16x16x32_bf16 v[56:59], a[20:23], a[92:95], v[56:59]\n"
      "buffer_load_dwordx4 a[8:11], v254, s[12:15], s63 offen sc1\n"
      "v_mfma_f32_16x16x32_bf16 v[20:23], a[16:19], a[92:95], v[20:23]\n"
      "ds_read_b128 a[136:139], v121 offset:32768\n"
      "v_mfma_f32_16x16x32_bf16 v[24:27], a[16:19], a[96:99], v[24:27]\n"
      "ds_read_b128 a[140:143], v121 offset:34816\n"
      "v_mfma_f32_16x16x32_bf16 v[60:63], a[20:23], a[96:99], v[60:63]\n"
      "buffer_store_dwordx4 v[224:227], v254, s[12:15], s53 offen offset:0 sc1\n"
      "v_mfma_f32_16x16x32_bf16 v[64:67], a[20:23], a[100:103], v[64:67]\n"
      "s_add_u32 m0, s74, 88064\n"
      "s_nop 0\n"
      "buffer_load_dwordx4 v107, s[4:7], s76 offen offset:0 lds\n"
      "v_mfma_f32_16x16x32_bf16 v[28:31], a[16:19], a[100:103], v[28:31]\n"
      "v_mfma_f32_16x16x32_bf16 v[32:35], a[16:19], a[104:107], v[32:35]\n"
      "buffer_load_dwordx4 a[12:15], v126, s[12:15], s63 offen offset:1024 sc1\n"
      "s_add_u32 s63, s63, 4096\n"
      "v_mfma_f32_16x16x32_bf16 v[68:71], a[20:23], a[104:107], v[68:71]\n"
      "s_waitcnt vmcnt(13) lgkmcnt(0)\n"
      "v_mfma_f32_16x16x32_bf16 v[0:3], a[24:27], a[108:111], v[0:3]\n"
      "s_barrier\n"
      "v_mfma_f32_16x16x32_bf16 v[36:39], a[28:31], a[108:111], v[36:39]\n"
      "ds_read_b128 a[72:75], v120 offset:36864\n"
      "v_mfma_f32_16x16x32_bf16 v[40:43], a[28:31], a[112:115], v[40:43]\n"
      "buffer_load_dwordx4 a[16:19], v254, s[12:15], s63 offen sc1\n"
      "v_mfma_f32_16x16x32_bf16 v[4:7], a[24:27], a[112:115], v[4:7]\n"
      "ds_read_b128 a[76:79], v120 offset:38912\n"
      "v_mfma_f32_16x16x32_bf16 v[8:11], a[24:27], a[116:119], v[8:11]\n"
      "ds_read_b128 a[80:83], v120 offset:40960\n"
      "v_mfma_f32_16x16x32_bf16 v[44:47], a[28:31], a[116:119], v[44:47]\n"
      "ds_read_b128 a[84:87], v120 offset:43008\n"
      "v_mfma_f32_16x16x32_bf16 v[48:51], a[28:31], a[120:123], v[48:51]\n"
      "buffer_load_dwordx4 v108, s[4:7], s76 offen offset:2048 lds\n"
      "s_add_u32 s76, s76, 256\n"
      "v_mfma_f32_16x16x32_bf16 v[12:15], a[24:27], a[120:123], v[12:15]\n"
      "ds_read_b128 a[88:91], v120 offset:45056\n"
      "s_cmp_ge_u32 s76, 1280\n"
      "s_cselect_b32 s49, 1280, 0\n"
      "v_mfma_f32_16x16x32_bf16 v[16:19], a[24:27], a[124:127], v[16:19]\n"
      "ds_read_b128 a[92:95], v120 offset:47104\n"
      "s_sub_u32 s76, s76, s49\n"
      "v_mfma_f32_16x16x32_bf16 v[52:55], a[28:31], a[124:127], v[52:55]\n"
      "ds_read_b128 a[96:99], v120 offset:49152\n"
      "v_mfma_f32_16x16x32_bf16 v[56:59], a[28:31], a[128:131], v[56:59]\n"
      "buffer_load_dwordx4 a[20:23], v126, s[12:15], s63 offen offset:1024 sc1\n"
      "s_add_u32 s63, s63, 4096\n"
      "v_mfma_f32_16x16x32_bf16 v[20:23], a[24:27], a[128:131], v[20:23]\n"
      "ds_read_b128 a[100:103], v120 offset:51200\n"
      "s_cmp_eq_u32 s63, s91\n"
      "s_cselect_b32 s63, s56, s63\n"
      "v_mfma_f32_16x16x32_bf16 v[24:27], a[24:27], a[132:135], v[24:27]\n"
      "ds_read_b128 a[104:107], v120 offset:53248\n"
      "v_mfma_f32_16x16x32_bf16 v[60:63], a[28:31], a[132:135], v[60:63]\n"
      "buffer_store_dwordx4 v[248:251], v207, s[12:15], s54 offen sc1\n"
      "v_mfma_f32_16x16x32_bf16 v[64:67], a[28:31], a[136:139], v[64:67]\n"
      "s_add_u32 m0, s74, 110592\n"
      "s_mov_b32 exec_lo, s78\n"
      "buffer_load_dwordx4 v100, s[4:7], s76 offen lds\n"
      "s_mov_b32 exec_lo, -1\n"
      "v_mfma_f32_16x16x32_bf16 v[28:31], a[24:27], a[136:139], v[28:31]\n"
      "v_mfma_f32_16x16x32_bf16 v[32:35], a[24:27], a[140:143], v[32:35]\n"
      "v_mfma_f32_16x16x32_bf16 v[68:71], a[28:31], a[140:143], v[68:71]\n"
      "s_waitcnt vmcnt(14) lgkmcnt(0)\n"
      "v_mfma_f32_16x16x32_bf16 v[0:3], a[32:35], a[72:75], v[0:3]\n"
      "v_mfma_f32_16x16x32_bf16 v[36:39], a[36:39], a[72:75], v[36:39]\n"
      "ds_read_b128 a[108:111], v121 offset:36864\n"
      "v_mfma_f32_16x16x32_bf16 v[40:43], a[36:39], a[76:79], v[40:43]\n"
      "buffer_load_dwordx4 a[24:27], v254, s[12:15], s63 offen sc1\n"
      "v_mfma_f32_16x16x32_bf16 v[4:7], a[32:35], a[76:79], v[4:7]\n"
      "ds_read_b128 a[112:115], v121 offset:38912\n"
      "v_mfma_f32_16x16x32_bf16 v[8:11], a[32:35], a[80:83], v[8:11]\n"
      "ds_read_b128 a[116:119], v121 offset:40960\n"
      "v_mfma_f32_16x16x32_bf16 v[44:47], a[36:39], a[80:83], v[44:47]\n"
      "ds_read_b128 a[120:123], v121 offset:43008\n"
      "v_mfma_f32_16x16x32_bf16 v[48:51], a[36:39], a[84:87], v[48:51]\n"
      "s_add_u32 m0, s74, 112640\n"
      "s_nop 0\n"
      "buffer_load_dwordx4 v101, s[4:7], s76 offen offset:0 lds\n"
      "v_mfma_f32_16x16x32_bf16 v[12:15], a[32:35], a[84:87], v[12:15]\n"
      "ds_read_b128 a[124:127], v121 offset:45056\n"
      "v_mfma_f32_16x16x32_bf16 v[16:19], a[32:35], a[88:91], v[16:19]\n"
      "ds_read_b128 a[128:131], v121 offset:47104\n"
      "v_mfma_f32_16x16x32_bf16 v[52:55], a[36:39], a[88:91], v[52:55]\n"
      "ds_read_b128 a[132:135], v121 offset:49152\n"
      "v_mfma_f32_16x16x32_bf16 v[56:59], a[36:39], a[92:95], v[56:59]\n"
      "buffer_load_dwordx4 a[28:31], v126, s[12:15], s63 offen offset:1024 sc1\n"
      "s_add_u32 s63, s63, 4096\n"
      "v_mfma_f32_16x16x32_bf16 v[20:23], a[32:35], a[92:95], v[20:23]\n"
      "ds_read_b128 a[136:139], v121 offset:51200\n"
      "v_mfma_f32_16x16x32_bf16 v[24:27], a[32:35], a[96:99], v[24:27]\n"
      "ds_read_b128 a[140:143], v121 offset:53248\n"
      "v_mfma_f32_16x16x32_bf16 v[60:63], a[36:39], a[96:99], v[60:63]\n"
      "v_mfma_f32_16x16x32_bf16 v[64:67], a[36:39], a[100:103], v[64:67]\n"
      "buffer_load_dwordx4 v102, s[4:7], s76 offen offset:2048 lds\n"
      "v_mfma_f32_16x16x32_bf16 v[28:31], a[32:35], a[100:103], v[28:31]\n"
      "v_mfma_f32_16x16x32_bf16 v[32:35], a[32:35], a[104:107], v[32:35]\n"
      "v_mfma_f32_16x16x32_bf16 v[68:71], a[36:39], a[104:107], v[68:71]\n"
      "s_waitcnt vmcnt(14) lgkmcnt(0)\n"
      "v_mfma_f32_16x16x32_bf16 v[0:3], a[0:3], a[108:111], v[0:3]\n"
      "v_mfma_f32_16x16x32_bf16 v[36:39], a[4:7], a[108:111], v[36:39]\n"
      "ds_read_b128 a[72:75], v122 offset:0\n"
      "v_mfma_f32_16x16x32_bf16 v[40:43], a[4:7], a[112:115], v[40:43]\n"
      "buffer_load_dwordx4 a[32:35], v254, s[12:15], s63 offen sc1\n"
      "v_mfma_f32_16x16x32_bf16 v[4:7], a[0:3], a[112:115], v[4:7]\n"
      "ds_read_b128 a[76:79], v122 offset:2048\n"
      "v_mfma_f32_16x16x32_bf16 v[8:11], a[0:3], a[116:119], v[8:11]\n"
      "ds_read_b128 a[80:83], v122 offset:4096\n"
      "v_mfma_f32_16x16x32_bf16 v[44:47], a[4:7], a[116:119], v[44:47]\n"
      "ds_read_b128 a[84:87], v122 offset:6144\n"
      "v_mfma_f32_16x16x32_bf16 v[48:51], a[4:7], a[120:123], v[48:51]\n"
      "s_add_u32 m0, s74, 116736\n"
      "s_nop 0\n"
      "buffer_load_dwordx4 v103, s[4:7], s76 offen offset:0 lds\n"
      "v_mfma_f32_16x16x32_bf16 v[12:15], a[0:3], a[120:123], v[12:15]\n"
      "ds_read_b128 a[88:91], v122 offset:8192\n"
      "v_mfma_f32_16x16x32_bf16 v[16:19], a[0:3], a[124:127], v[16:19]\n"
      "ds_read_b128 a[92:95], v122 offset:10240\n"
      "v_mfma_f32_16x16x32_bf16 v[52:55], a[4:7], a[124:127], v[52:55]\n"
      "ds_read_b128 a[96:99], v122 offset:12288\n"
      "v_mfma_f32_16x16x32_bf16 v[56:59], a[4:7], a[128:131], v[56:59]\n"
      "buffer_load_dwordx4 a[36:39], v126, s[12:15], s63 offen offset:1024 sc1\n"
      "s_add_u32 s63, s63, 4096\n"
      "v_mfma_f32_16x16x32_bf16 v[20:23], a[0:3], a[128:131], v[20:23]\n"
      "ds_read_b128 a[100:103], v122 offset:14336\n"
      "s_cmp_eq_u32 s63, s91\n"
      "s_cselect_b32 s63, s56, s63\n"
      "v_mfma_f32_16x16x32_bf16 v[24:27], a[0:3], a[132:135], v[24:27]\n"
      "ds_read_b128 a[104:107], v122 offset:16384\n"
      "v_mfma_f32_16x16x32_bf16 v[60:63], a[4:7], a[132:135], v[60:63]\n"
      "v_mfma_f32_16x16x32_bf16 v[64:67], a[4:7], a[136:139], v[64:67]\n"
      "buffer_load_dwordx4 v104, s[4:7], s76 offen offset:2048 lds\n"
      "v_mfma_f32_16x16x32_bf16 v[28:31], a[0:3], a[136:139], v[28:31]\n"
      "v_mfma_f32_16x16x32_bf16 v[32:35], a[0:3], a[140:143], v[32:35]\n"
      "v_mfma_f32_16x16x32_bf16 v[68:71], a[4:7], a[140:143], v[68:71]\n"
      "s_waitcnt vmcnt(13) lgkmcnt(0)\n"
      "v_mfma_f32_16x16x32_bf16 v[0:3], a[8:11], a[72:75], v[0:3]\n"
      "v_mfma_f32_16x16x32_bf16 v[36:39], a[12:15], a[72:75], v[36:39]\n"
      "ds_read_b128 a[108:111], v123 offset:0\n"
      "v_mfma_f32_16x16x32_bf16 v[40:43], a[12:15], a[76:79], v[40:43]\n"
      "buffer_load_dwordx4 a[0:3], v254, s[12:15], s63 offen sc1\n"
      "v_mfma_f32_16x16x32_bf16 v[4:7], a[8:11], a[76:79], v[4:7]\n"
      "ds_read_b128 a[112:115], v123 offset:2048\n"
      "v_mfma_f32_16x16x32_bf16 v[8:11], a[8:11], a[80:83], v[8:11]\n"
      "ds_read_b128 a[116:119], v123 offset:4096\n"
      "v_mfma_f32_16x16x32_bf16 v[44:47], a[12:15], a[80:83], v[44:47]\n"
      "ds_read_b128 a[120:123], v123 offset:6144\n"
      "v_mfma_f32_16x16x32_bf16 v[48:51], a[12:15], a[84:87], v[48:51]\n"
      "s_add_u32 m0, s74, 120832\n"
      "s_nop 0\n"
      "buffer_load_dwordx4 v105, s[4:7], s76 offen offset:0 lds\n"
      "v_mfma_f32_16x16x32_bf16 v[12:15], a[8:11], a[84:87], v[12:15]\n"
      "ds_read_b128 a[124:127], v123 offset:8192\n"
      "v_mfma_f32_16x16x32_bf16 v[16:19], a[8:11], a[88:91], v[16:19]\n"
      "ds_read_b128 a[128:131], v123 offset:10240\n"
      "v_mfma_f32_16x16x32_bf16 v[52:55], a[12:15], a[88:91], v[52:55]\n"
      "ds_read_b128 a[132:135], v123 offset:12288\n"
      "v_mfma_f32_16x16x32_bf16 v[56:59], a[12:15], a[92:95], v[56:59]\n"
      "buffer_load_dwordx4 a[4:7], v126, s[12:15], s63 offen offset:1024 sc1\n"
      "s_add_u32 s63, s63, 4096\n"
      "v_mfma_f32_16x16x32_bf16 v[20:23], a[8:11], a[92:95], v[20:23]\n"
      "ds_read_b128 a[136:139], v123 offset:14336\n"
      "v_mfma_f32_16x16x32_bf16 v[24:27], a[8:11], a[96:99], v[24:27]\n"
      "ds_read_b128 a[140:143], v123 offset:16384\n"
      "v_mfma_f32_16x16x32_bf16 v[60:63], a[12:15], a[96:99], v[60:63]\n"
      "v_mfma_f32_16x16x32_bf16 v[64:67], a[12:15], a[100:103], v[64:67]\n"
      "buffer_load_dwordx4 v106, s[4:7], s76 offen offset:2048 lds\n"
      "v_mfma_f32_16x16x32_bf16 v[28:31], a[8:11], a[100:103], v[28:31]\n"
      "v_mfma_f32_16x16x32_bf16 v[32:35], a[8:11], a[104:107], v[32:35]\n"
      "v_mfma_f32_16x16x32_bf16 v[68:71], a[12:15], a[104:107], v[68:71]\n"
      "s_waitcnt vmcnt(14) lgkmcnt(0)\n"
      "v_mfma_f32_16x16x32_bf16 v[0:3], a[16:19], a[108:111], v[0:3]\n"
      "s_waitcnt vmcnt(13)\n"
      "s_barrier\n"
      "v_mfma_f32_16x16x32_bf16 v[36:39], a[20:23], a[108:111], v[36:39]\n"
      "ds_read_b128 a[72:75], v122 offset:18432\n"
      "s_mov_b64 exec, 0xff\n"
      "buffer_store_dwordx4 v[196:199], v200, s[12:15], s43 offen sc1\n"
      "s_mov_b64 exec, -1\n"
      "v_mfma_f32_16x16x32_bf16 v[40:43], a[20:23], a[112:115], v[40:43]\n"
      "buffer_load_dwordx4 a[8:11], v254, s[12:15], s63 offen sc1\n"
      "v_mfma_f32_16x16x32_bf16 v[4:7], a[16:19], a[112:115], v[4:7]\n"
      "ds_read_b128 a[76:79], v122 offset:20480\n"
      "v_mfma_f32_16x16x32_bf16 v[8:11], a[16:19], a[116:119], v[8:11]\n"
      "ds_read_b128 a[80:83], v122 offset:22528\n"
      "v_mfma_f32_16x16x32_bf16 v[44:47], a[20:23], a[116:119], v[44:47]\n"
      "ds_read_b128 a[84:87], v122 offset:24576\n"
      "v_mfma_f32_16x16x32_bf16 v[48:51], a[20:23], a[120:123], v[48:51]\n"
      "s_add_u32 m0, s74, 124928\n"
      "s_nop 0\n"
      "buffer_load_dwordx4 v107, s[4:7], s76 offen offset:0 lds\n"
      "v_mfma_f32_16x16x32_bf16 v[12:15], a[16:19], a[120:123], v[12:15]\n"
      "ds_read_b128 a[88:91], v122 offset:26624\n"
      "v_mfma_f32_16x16x32_bf16 v[16:19], a[16:19], a[124:127], v[16:19]\n"
      "ds_read_b128 a[92:95], v122 offset:28672\n"
      "v_mfma_f32_16x16x32_bf16 v[52:55], a[20:23], a[124:127], v[52:55]\n"
      "ds_read_b128 a[96:99], v122 offset:30720\n"
      "v_mfma_f32_16x16x32_bf16 v[56:59], a[20:23], a[128:131], v[56:59]\n"
      "buffer_load_dwordx4 a[12:15], v126, s[12:15], s63 offen offset:1024 sc1\n"
      "s_add_u32 s63, s63, 4096\n"
      "v_mfma_f32_16x16x32_bf16 v[20:23], a[16:19], a[128:131], v[20:23]\n"
      "ds_read_b128 a[100:103], v122 offset:32768\n"
      "s_cmp_eq_u32 s63, s91\n"
      "s_cselect_b32 s63, s56, s63\n"
      "v_mfma_f32_16x16x32_bf16 v[24:27], a[16:19], a[132:135], v[24:27]\n"
      "ds_read_b128 a[104:107], v122 offset:34816\n"
      "v_mfma_f32_16x16x32_bf16 v[60:63], a[20:23], a[132:135], v[60:63]\n"
      "v_mfma_f32_16x16x32_bf16 v[64:67], a[20:23], a[136:139], v[64:67]\n"
      "buffer_load_dwordx4 v108, s[4:7], s76 offen offset:2048 lds\n"
      "s_add_u32 s76, s76, 256\n"
      "v_mfma_f32_16x16x32_bf16 v[28:31], a[16:19], a[136:139], v[28:31]\n"
      "s_cmp_ge_u32 s76, 1280\n"
      "s_cselect_b32 s49, 1280, 0\n"
      "v_mfma_f32_16x16x32_bf16 v[32:35], a[16:19], a[140:143], v[32:35]\n"
      "s_sub_u32 s76, s76, s49\n"
      "v_mfma_f32_16x16x32_bf16 v[68:71], a[20:23], a[140:143], v[68:71]\n"
      "s_waitcnt vmcnt(14) lgkmcnt(0)\n"
      "v_mfma_f32_16x16x32_bf16 v[0:3], a[24:27], a[72:75], v[0:3]\n"
      "v_mfma_f32_16x16x32_bf16 v[36:39], a[28:31], a[72:75], v[36:39]\n"
      "ds_read_b128 a[108:111], v123 offset:18432\n"
      "v_mfma_f32_16x16x32_bf16 v[40:43], a[28:31], a[76:79], v[40:43]\n"
      "buffer_load_dwordx4 a[16:19], v254, s[12:15], s63 offen sc1\n"
      "v_mfma_f32_16x16x32_bf16 v[4:7], a[24:27], a[76:79], v[4:7]\n"
      "ds_read_b128 a[112:115], v123 offset:20480\n"
      "v_mfma_f32_16x16x32_bf16 v[8:11], a[24:27], a[80:83], v[8:11]\n"
      "ds_read_b128 a[116:119], v123 offset:22528\n"
      "v_mfma_f32_16x16x32_bf16 v[44:47], a[28:31], a[80:83], v[44:47]\n"
      "ds_read_b128 a[120:123], v123 offset:24576\n"
      "v_mfma_f32_16x16x32_bf16 v[48:51], a[28:31], a[84:87], v[48:51]\n"
      "s_add_u32 m0, s74, 0\n"
      "s_mov_b32 exec_lo, s78\n"
      "buffer_load_dwordx4 v100, s[4:7], s76 offen lds\n"
      "s_mov_b32 exec_lo, -1\n"
      "v_mfma_f32_16x16x32_bf16 v[12:15], a[24:27], a[84:87], v[12:15]\n"
      "ds_read_b128 a[124:127], v123 offset:26624\n"
      "v_mfma_f32_16x16x32_bf16 v[16:19], a[24:27], a[88:91], v[16:19]\n"
      "ds_read_b128 a[128:131], v123 offset:28672\n"
      "v_mfma_f32_16x16x32_bf16 v[52:55], a[28:31], a[88:91], v[52:55]\n"
      "ds_read_b128 a[132:135], v123 offset:30720\n"
      "v_mfma_f32_16x16x32_bf16 v[56:59], a[28:31], a[92:95], v[56:59]\n"
      "buffer_load_dwordx4 a[20:23], v126, s[12:15], s63 offen offset:1024 sc1\n"
      "s_add_u32 s63, s63, 4096\n"
      "v_mfma_f32_16x16x32_bf16 v[20:23], a[24:27], a[92:95], v[20:23]\n"
      "ds_read_b128 a[136:139], v123 offset:32768\n"
      "v_mfma_f32_16x16x32_bf16 v[24:27], a[24:27], a[96:99], v[24:27]\n"
      "ds_read_b128 a[140:143], v123 offset:34816\n"
      "v_mfma_f32_16x16x32_bf16 v[60:63], a[28:31], a[96:99], v[60:63]\n"
      "v_mfma_f32_16x16x32_bf16 v[64:67], a[28:31], a[100:103], v[64:67]\n"
      "s_add_u32 m0, s74, 2048\n"
      "s_nop 0\n"
      "buffer_load_dwordx4 v101, s[4:7], s76 offen offset:0 lds\n"
      "v_mfma_f32_16x16x32_bf16 v[28:31], a[24:27], a[100:103], v[28:31]\n"
      "v_mfma_f32_16x16x32_bf16 v[32:35], a[24:27], a[104:107], v[32:35]\n"
      "v_mfma_f32_16x16x32_bf16 v[68:71], a[28:31], a[104:107], v[68:71]\n"
      "s_waitcnt vmcnt(14) lgkmcnt(0)\n"
      "v_mfma_f32_16x16x32_bf16 v[0:3], a[32:35], a[108:111], v[0:3]\n"
      "v_mfma_f32_16x16x32_bf16 v[36:39], a[36:39], a[108:111], v[36:39]\n"
      "ds_read_b128 a[72:75], v122 offset:36864\n"
      "v_mfma_f32_16x16x32_bf16 v[40:43], a[36:39], a[112:115], v[40:43]\n"
      "buffer_load_dwordx4 a[24:27], v254, s[12:15], s63 offen sc1\n"
      "v_mfma_f32_16x16x32_bf16 v[4:7], a[32:35], a[112:115], v[4:7]\n"
      "ds_read_b128 a[76:79], v122 offset:38912\n"
      "v_mfma_f32_16x16x32_bf16 v[8:11], a[32:35], a[116:119], v[8:11]\n"
      "ds_read_b128 a[80:83], v122 offset:40960\n"
      "v_mfma_f32_16x16x32_bf16 v[44:47], a[36:39], a[116:119], v[44:47]\n"
      "ds_read_b128 a[84:87], v122 offset:43008\n"
      "v_mfma_f32_16x16x32_bf16 v[48:51], a[36:39], a[120:123], v[48:51]\n"
      "buffer_load_dwordx4 v102, s[4:7], s76 offen offset:2048 lds\n"
      "v_mfma_f32_16x16x32_bf16 v[12:15], a[32:35], a[120:123], v[12:15]\n"
      "ds_read_b128 a[88:91], v122 offset:45056\n"
      "v_mfma_f32_16x16x32_bf16 v[16:19], a[32:35], a[124:127], v[16:19]\n"
      "ds_read_b128 a[92:95], v122 offset:47104\n"
      "v_mfma_f32_16x16x32_bf16 v[52:55], a[36:39], a[124:127], v[52:55]\n"
      "ds_read_b128 a[96:99], v122 offset:49152\n"
      "v_mfma_f32_16x16x32_bf16 v[56:59], a[36:39], a[128:131], v[56:59]\n"
      "buffer_load_dwordx4 a[28:31], v126, s[12:15], s63 offen offset:1024 sc1\n"
      "s_add_u32 s63, s63, 4096\n"
      "v_mfma_f32_16x16x32_bf16 v[20:23], a[32:35], a[128:131], v[20:23]\n"
      "ds_read_b128 a[100:103], v122 offset:51200\n"
      "s_cmp_eq_u32 s63, s91\n"
      "s_cselect_b32 s63, s56, s63\n"
      "v_mfma_f32_16x16x32_bf16 v[24:27], a[32:35], a[132:135], v[24:27]\n"
      "ds_read_b128 a[104:107], v122 offset:53248\n"
      "v_mfma_f32_16x16x32_bf16 v[60:63], a[36:39], a[132:135], v[60:63]\n"
      "v_mfma_f32_16x16x32_bf16 v[64:67], a[36:39], a[136:139], v[64:67]\n"
      "s_add_u32 m0, s74, 6144\n"
      "s_nop 0\n"
      "buffer_load_dwordx4 v103, s[4:7], s76 offen offset:0 lds\n"
      "v_mfma_f32_16x16x32_bf16 v[28:31], a[32:35], a[136:139], v[28:31]\n"
      "v_mfma_f32_16x16x32_bf16 v[32:35], a[32:35], a[140:143], v[32:35]\n"
      "buffer_load_dwordx4 v104, s[4:7], s76 offen offset:2048 lds\n"
      "v_mfma_f32_16x16x32_bf16 v[68:71], a[36:39], a[140:143], v[68:71]\n"
      "s_waitcnt vmcnt(15) lgkmcnt(0)\n"
      "v_mfma_f32_16x16x32_bf16 v[0:3], a[0:3], a[72:75], v[0:3]\n"
      "v_mfma_f32_16x16x32_bf16 v[36:39], a[4:7], a[72:75], v[36:39]\n"
      "ds_read_b128 a[108:111], v123 offset:36864\n"
      "v_mfma_f32_16x16x32_bf16 v[40:43], a[4:7], a[76:79], v[40:43]\n"
      "buffer_load_dwordx4 a[32:35], v254, s[12:15], s63 offen sc1\n"
      "v_mfma_f32_16x16x32_bf16 v[4:7], a[0:3], a[76:79], v[4:7]\n"
      "ds_read_b128 a[112:115], v123 offset:38912\n"
      "v_mfma_f32_16x16x32_bf16 v[8:11], a[0:3], a[80:83], v[8:11]\n"
      "ds_read_b128 a[116:119], v123 offset:40960\n"
      "v_mfma_f32_16x16x32_bf16 v[44:47], a[4:7], a[80:83], v[44:47]\n"
      "ds_read_b128 a[120:123], v123 offset:43008\n"
      "v_mfma_f32_16x16x32_bf16 v[48:51], a[4:7], a[84:87], v[48:51]\n"
      "s_add_u32 m0, s74, 10240\n"
      "s_nop 0\n"
      "buffer_load_dwordx4 v105, s[4:7], s76 offen offset:0 lds\n"
      "v_mfma_f32_16x16x32_bf16 v[12:15], a[0:3], a[84:87], v[12:15]\n"
      "ds_read_b128 a[124:127], v123 offset:45056\n"
      "v_mfma_f32_16x16x32_bf16 v[16:19], a[0:3], a[88:91], v[16:19]\n"
      "ds_read_b128 a[128:131], v123 offset:47104\n"
      "v_mfma_f32_16x16x32_bf16 v[52:55], a[4:7], a[88:91], v[52:55]\n"
      "ds_read_b128 a[132:135], v123 offset:49152\n"
      "v_mfma_f32_16x16x32_bf16 v[56:59], a[4:7], a[92:95], v[56:59]\n"
      "buffer_load_dwordx4 a[36:39], v126, s[12:15], s63 offen offset:1024 sc1\n"
      "s_add_u32 s63, s63, 4096\n"
      "v_mfma_f32_16x16x32_bf16 v[20:23], a[0:3], a[92:95], v[20:23]\n"
      "ds_read_b128 a[136:139], v123 offset:51200\n"
      "v_mfma_f32_16x16x32_bf16 v[24:27], a[0:3], a[96:99], v[24:27]\n"
      "ds_read_b128 a[140:143], v123 offset:53248\n"
      "v_mfma_f32_16x16x32_bf16 v[60:63], a[4:7], a[96:99], v[60:63]\n"
      "v_mfma_f32_16x16x32_bf16 v[64:67], a[4:7], a[100:103], v[64:67]\n"
      "buffer_load_dwordx4 v106, s[4:7], s76 offen offset:2048 lds\n"
      "v_mfma_f32_16x16x32_bf16 v[28:31], a[0:3], a[100:103], v[28:31]\n"
      "v_mfma_f32_16x16x32_bf16 v[32:35], a[0:3], a[104:107], v[32:35]\n"
      "v_mfma_f32_16x16x32_bf16 v[68:71], a[4:7], a[104:107], v[68:71]\n"
      "s_waitcnt vmcnt(14) lgkmcnt(0)\n"
      "v_mfma_f32_16x16x32_bf16 v[0:3], a[8:11], a[108:111], v[0:3]\n"
      "s_waitcnt vmcnt(13)\n"
      "s_barrier\n"
      "v_mfma_f32_16x16x32_bf16 v[36:39], a[12:15], a[108:111], v[36:39]\n"
      "ds_read_b128 a[72:75], v124 offset:0\n"
      "v_mfma_f32_16x16x32_bf16 v[40:43], a[12:15], a[112:115], v[40:43]\n"
      "buffer_load_dwordx4 a[0:3], v254, s[12:15], s63 offen sc1\n"
      "v_mfma_f32_16x16x32_bf16 v[4:7], a[8:11], a[112:115], v[4:7]\n"
      "ds_read_b128 a[76:79], v124 offset:2048\n"
      "v_mfma_f32_16x16x32_bf16 v[8:11], a[8:11], a[116:119], v[8:11]\n"
      "ds_read_b128 a[80:83], v124 offset:4096\n"
      "v_mfma_f32_16x16x32_bf16 v[44:47], a[12:15], a[116:119], v[44:47]\n"
      "ds_read_b128 a[84:87], v124 offset:6144\n"
      "v_mfma_f32_16x16x32_bf16 v[48:51], a[12:15], a[120:123], v[48:51]\n"
      "s_add_u32 m0, s74, 14336\n"
      "s_nop 0\n"
      "buffer_load_dwordx4 v107, s[4:7], s76 offen offset:0 lds\n"
      "v_mfma_f32_16x16x32_bf16 v[12:15], a[8:11], a[120:123], v[12:15]\n"
      "ds_read_b128 a[88:91], v124 offset:8192\n"
      "v_mfma_f32_16x16x32_bf16 v[16:19], a[8:11], a[124:127], v[16:19]\n"
      "ds_read_b128 a[92:95], v124 offset:10240\n"
      "v_mfma_f32_16x16x32_bf16 v[52:55], a[12:15], a[124:127], v[52:55]\n"
      "ds_read_b128 a[96:99], v124 offset:12288\n"
      "v_mfma_f32_16x16x32_bf16 v[56:59], a[12:15], a[128:131], v[56:59]\n"
      "buffer_load_dwordx4 a[4:7], v126, s[12:15], s63 offen offset:1024 sc1\n"
      "s_add_u32 s63, s63, 4096\n"
      "v_mfma_f32_16x16x32_bf16 v[20:23], a[8:11], a[128:131], v[20:23]\n"
      "ds_read_b128 a[100:103], v124 offset:14336\n"
      "s_cmp_eq_u32 s63, s91\n"
      "s_cselect_b32 s63, s56, s63\n"
      "v_mfma_f32_16x16x32_bf16 v[24:27], a[8:11], a[132:135], v[24:27]\n"
      "ds_read_b128 a[104:107], v124 offset:16384\n"
      "v_mfma_f32_16x16x32_bf16 v[60:63], a[12:15], a[132:135], v[60:63]\n"
      "v_mfma_f32_16x16x32_bf16 v[64:67], a[12:15], a[136:139], v[64:67]\n"
      "buffer_load_dwordx4 v108, s[4:7], s76 offen offset:2048 lds\n"
      "v_mfma_f32_16x16x32_bf16 v[28:31], a[8:11], a[136:139], v[28:31]\n"
      "v_mfma_f32_16x16x32_bf16 v[32:35], a[8:11], a[140:143], v[32:35]\n"
      "v_mfma_f32_16x16x32_bf16 v[68:71], a[12:15], a[140:143], v[68:71]\n"
      "s_waitcnt vmcnt(14) lgkmcnt(0)\n"
      "v_mfma_f32_16x16x32_bf16 v[0:3], a[16:19], a[72:75], v[0:3]\n"
      "v_mfma_f32_16x16x32_bf16 v[36:39], a[20:23], a[72:75], v[36:39]\n"
      "ds_read_b128 a[108:111], v125 offset:0\n"
      "v_mfma_f32_16x16x32_bf16 v[40:43], a[20:23], a[76:79], v[40:43]\n"
      "buffer_load_dwordx4 a[8:11], v254, s[12:15], s63 offen sc1\n"
      "v_mfma_f32_16x16x32_bf16 v[4:7], a[16:19], a[76:79], v[4:7]\n"
      "ds_read_b128 a[112:115], v125 offset:2048\n"
      "v_mfma_f32_16x16x32_bf16 v[8:11], a[16:19], a[80:83], v[8:11]\n"
      "ds_read_b128 a[116:119], v125 offset:4096\n"
      "v_mfma_f32_16x16x32_bf16 v[44:47], a[20:23], a[80:83], v[44:47]\n"
      "ds_read_b128 a[120:123], v125 offset:6144\n"
      "v_mfma_f32_16x16x32_bf16 v[48:51], a[20:23], a[84:87], v[48:51]\n"
      "buffer_load_dwordx4 a[12:15], v126, s[12:15], s63 offen offset:1024 sc1\n"
      "s_add_u32 s63, s63, 4096\n"
      "v_mfma_f32_16x16x32_bf16 v[12:15], a[16:19], a[84:87], v[12:15]\n"
      "ds_read_b128 a[124:127], v125 offset:8192\n"
      "v_mfma_f32_16x16x32_bf16 v[16:19], a[16:19], a[88:91], v[16:19]\n"
      "ds_read_b128 a[128:131], v125 offset:10240\n"
      "v_mfma_f32_16x16x32_bf16 v[52:55], a[20:23], a[88:91], v[52:55]\n"
      "ds_read_b128 a[132:135], v125 offset:12288\n"
      "v_mfma_f32_16x16x32_bf16 v[56:59], a[20:23], a[92:95], v[56:59]\n"
      "v_mfma_f32_16x16x32_bf16 v[20:23], a[16:19], a[92:95], v[20:23]\n"
      "ds_read_b128 a[136:139], v125 offset:14336\n"
      "v_mfma_f32_16x16x32_bf16 v[24:27], a[16:19], a[96:99], v[24:27]\n"
      "ds_read_b128 a[140:143], v125 offset:16384\n"
      "v_mfma_f32_16x16x32_bf16 v[60:63], a[20:23], a[96:99], v[60:63]\n"
      ".Lk_pad2:\n"
      "s_cbranch_execz .Lk_pad6\n"
      "v_mfma_f32_16x16x32_bf16 v[64:67], a[20:23], a[100:103], v[64:67]\n"
      "v_mfma_f32_16x16x32_bf16 v[28:31], a[16:19], a[100:103], v[28:31]\n"
      "buffer_load_dwordx4 v[232:235], v152, s[12:15], s72 offen sc1\n"
      "buffer_load_dwordx4 v[236:239], v153, s[12:15], s72 offen sc1\n"
      "buffer_load_dwordx4 v[228:231], v154, s[12:15], s72 offen sc1\n"
      "s_mov_b32 exec_lo, 0x3fff001f\n"
      "s_mov_b32 exec_hi, 0xf\n"
      "buffer_load_dwordx4 v[240:243], v204, s[12:15], s72 offen sc1\n"
      "s_mov_b64 exec, -1\n"
      "v_mfma_f32_16x16x32_bf16 v[32:35], a[16:19], a[104:107], v[32:35]\n"
      "v_mfma_f32_16x16x32_bf16 v[68:71], a[20:23], a[104:107], v[68:71]\n"
      "s_waitcnt vmcnt(16) lgkmcnt(0)\n"
      "v_mfma_f32_16x16x32_bf16 v[0:3], a[24:27], a[108:111], v[0:3]\n"
      "v_mfma_f32_16x16x32_bf16 v[36:39], a[28:31], a[108:111], v[36:39]\n"
      "ds_read_b128 a[72:75], v124 offset:18432\n"
      "v_mfma_f32_16x16x32_bf16 v[40:43], a[28:31], a[112:115], v[40:43]\n"
      "buffer_load_dwordx4 a[16:19], v254, s[12:15], s63 offen sc1\n"
      "v_mfma_f32_16x16x32_bf16 v[4:7], a[24:27], a[112:115], v[4:7]\n"
      "ds_read_b128 a[76:79], v124 offset:20480\n"
      "v_mfma_f32_16x16x32_bf16 v[8:11], a[24:27], a[116:119], v[8:11]\n"
      "ds_read_b128 a[80:83], v124 offset:22528\n"
      "v_mfma_f32_16x16x32_bf16 v[44:47], a[28:31], a[116:119], v[44:47]\n"
      "ds_read_b128 a[84:87], v124 offset:24576\n"
      "v_mfma_f32_16x16x32_bf16 v[48:51], a[28:31], a[120:123], v[48:51]\n"
      "buffer_load_dwordx4 a[20:23], v126, s[12:15], s63 offen offset:1024 sc1\n"
      "s_add_u32 s63, s63, 4096\n"
      "v_mfma_f32_16x16x32_bf16 v[12:15], a[24:27], a[120:123], v[12:15]\n"
      "ds_read_b128 a[88:91], v124 offset:26624\n"
      "s_cmp_eq_u32 s63, s91\n"
      "s_cselect_b32 s63, s56, s63\n"
      "v_mfma_f32_16x16x32_bf16 v[16:19], a[24:27], a[124:127], v[16:19]\n"
      "ds_read_b128 a[92:95], v124 offset:28672\n"
      "v_mfma_f32_16x16x32_bf16 v[52:55], a[28:31], a[124:127], v[52:55]\n"
      "ds_read_b128 a[96:99], v124 offset:30720\n"
      "v_mfma_f32_16x16x32_bf16 v[56:59], a[28:31], a[128:131], v[56:59]\n"
      "v_mfma_f32_16x16x32_bf16 v[20:23], a[24:27], a[128:131], v[20:23]\n"
      "ds_read_b128 a[100:103], v124 offset:32768\n"
      "v_mfma_f32_16x16x32_bf16 v[24:27], a[24:27], a[132:135], v[24:27]\n"
      "ds_read_b128 a[104:107], v124 offset:34816\n"
      "v_mfma_f32_16x16x32_bf16 v[60:63], a[28:31], a[132:135], v[60:63]\n"
      "v_mfma_f32_16x16x32_bf16 v[64:67], a[28:31], a[136:139], v[64:67]\n"
      "v_mfma_f32_16x16x32_bf16 v[28:31], a[24:27], a[136:139], v[28:31]\n"
      "v_mfma_f32_16x16x32_bf16 v[32:35], a[24:27], a[140:143], v[32:35]\n"
      "v_mfma_f32_16x16x32_bf16 v[68:71], a[28:31], a[140:143], v[68:71]\n"
      "s_waitcnt vmcnt(13) lgkmcnt(0)\n"
      "v_mfma_f32_16x16x32_bf16 v[0:3], a[32:35], a[72:75], v[0:3]\n"
      "v_mfma_f32_16x16x32_bf16 v[36:39], a[36:39], a[72:75], v[36:39]\n"
      "ds_read_b128 a[108:111], v125 offset:18432\n"
      "v_mfma_f32_16x16x32_bf16 v[40:43], a[36:39], a[76:79], v[40:43]\n"
      "buffer_load_dwordx4 a[24:27], v254, s[12:15], s63 offen sc1\n"
      "v_mfma_f32_16x16x32_bf16 v[4:7], a[32:35], a[76:79], v[4:7]\n"
      "ds_read_b128 a[112:115], v125 offset:20480\n"
      "v_mfma_f32_16x16x32_bf16 v[8:11], a[32:35], a[80:83], v[8:11]\n"
      "ds_read_b128 a[116:119], v125 offset:22528\n"
      "v_mfma_f32_16x16x32_bf16 v[44:47], a[36:39], a[80:83], v[44:47]\n"
      "ds_read_b128 a[120:123], v125 offset:24576\n"
      "v_mfma_f32_16x16x32_bf16 v[48:51], a[36:39], a[84:87], v[48:51]\n"
      "buffer_load_dwordx4 a[28:31], v126, s[12:15], s63 offen offset:1024 sc1\n"
      "s_add_u32 s63, s63, 4096\n"
      "v_mfma_f32_16x16x32_bf16 v[12:15], a[32:35], a[84:87], v[12:15]\n"
      "ds_read_b128 a[124:127], v125 offset:26624\n"
      "v_mfma_f32_16x16x32_bf16 v[16:19], a[32:35], a[88:91], v[16:19]\n"
      "ds_read_b128 a[128:131], v125 offset:28672\n"
      "v_mfma_f32_16x16x32_bf16 v[52:55], a[36:39], a[88:91], v[52:55]\n"
      "ds_read_b128 a[132:135], v125 offset:30720\n"
      "v_mfma_f32_16x16x32_bf16 v[56:59], a[36:39], a[92:95], v[56:59]\n"
      "v_mfma_f32_16x16x32_bf16 v[20:23], a[32:35], a[92:95], v[20:23]\n"
      "ds_read_b128 a[136:139], v125 offset:32768\n"
      "v_mfma_f32_16x16x32_bf16 v[24:27], a[32:35], a[96:99], v[24:27]\n"
      "ds_read_b128 a[140:143], v125 offset:34816\n"
      "v_mfma_f32_16x16x32_bf16 v[60:63], a[36:39], a[96:99], v[60:63]\n"
      "v_mfma_f32_16x16x32_bf16 v[64:67], a[36:39], a[100:103], v[64:67]\n"
      "v_mfma_f32_16x16x32_bf16 v[28:31], a[32:35], a[100:103], v[28:31]\n"
      "v_mfma_f32_16x16x32_bf16 v[32:35], a[32:35], a[104:107], v[32:35]\n"
      "v_mfma_f32_16x16x32_bf16 v[68:71], a[36:39], a[104:107], v[68:71]\n"
      "s_waitcnt vmcnt(11) lgkmcnt(0)\n"
      "v_mfma_f32_16x16x32_bf16 v[0:3], a[0:3], a[108:111], v[0:3]\n"
      "s_waitcnt vmcnt(10)\n"
      "s_barrier\n"
      "v_mfma_f32_16x16x32_bf16 v[36:39], a[4:7], a[108:111], v[36:39]\n"
      "ds_read_b128 a[72:75], v120 offset:0\n"
      "v_mfma_f32_16x16x32_bf16 v[40:43], a[4:7], a[112:115], v[40:43]\n"
      "buffer_load_dwordx4 a[32:35], v254, s[12:15], s63 offen sc1\n"
      "v_mfma_f32_16x16x32_bf16 v[4:7], a[0:3], a[112:115], v[4:7]\n"
      "ds_read_b128 a[76:79], v120 offset:2048\n"
      "v_mfma_f32_16x16x32_bf16 v[8:11], a[0:3], a[116:119], v[8:11]\n"
      "ds_read_b128 a[80:83], v120 offset:4096\n"
      "v_mfma_f32_16x16x32_bf16 v[44:47], a[4:7], a[116:119], v[44:47]\n"
      "ds_read_b128 a[84:87], v120 offset:6144\n"
      "v_mfma_f32_16x16x32_bf16 v[48:51], a[4:7], a[120:123], v[48:51]\n"
      "buffer_load_dwordx4 a[36:39], v126, s[12:15], s63 offen offset:1024 sc1\n"
      "v_mfma_f32_16x16x32_bf16 v[12:15], a[0:3], a[120:123], v[12:15]\n"
      "ds_read_b128 a[88:91], v120 offset:8192\n"
      "v_mfma_f32_16x16x32_bf16 v[16:19], a[0:3], a[124:127], v[16:19]\n"
      "ds_read_b128 a[92:95], v120 offset:10240\n"
      "v_mfma_f32_16x16x32_bf16 v[52:55], a[4:7], a[124:127], v[52:55]\n"
      "ds_read_b128 a[96:99], v120 offset:12288\n"
      "v_mfma_f32_16x16x32_bf16 v[56:59], a[4:7], a[128:131], v[56:59]\n"
      "v_mfma_f32_16x16x32_bf16 v[20:23], a[0:3], a[128:131], v[20:23]\n"
      "ds_read_b128 a[100:103], v120 offset:14336\n"
      "v_mfma_f32_16x16x32_bf16 v[24:27], a[0:3], a[132:135], v[24:27]\n"
      "ds_read_b128 a[104:107], v120 offset:16384\n"
      "v_mfma_f32_16x16x32_bf16 v[60:63], a[4:7], a[132:135], v[60:63]\n"
      "v_mfma_f32_16x16x32_bf16 v[64:67], a[4:7], a[136:139], v[64:67]\n"
      "v_mfma_f32_16x16x32_bf16 v[28:31], a[0:3], a[136:139], v[28:31]\n"
      "s_waitcnt vmcnt(8)\n"
      "v_bitop3_b32 v244, v197, v233, v237 bitop3:0x7e\n"
      "v_bitop3_b32 v245, v199, v235, v239 bitop3:0x7e\n"
      "s_waitcnt vmcnt(7)\n"
      "v_bitop3_b32 v244, v244, v197, v229 bitop3:0xf6\n"
      "v_bitop3_b32 v245, v245, v199, v231 bitop3:0xf6\n"
      "s_waitcnt vmcnt(6)\n"
      "v_bitop3_b32 v244, v244, v197, v241 bitop3:0xf6\n"
      "v_bitop3_b32 v245, v245, v199, v243 bitop3:0xf6\n"
      "v_cmp_ne_u64 vcc, 0, v[244:245]\n"
      "s_cbranch_vccnz .Lk_f2slow_4\n"
      ".Lk_f2back_5:\n"
      "v_mfma_f32_16x16x32_bf16 v[32:35], a[0:3], a[140:143], v[32:35]\n"
      "buffer_load_dwordx4 a[156:159], v254, s[12:15], s57 offen offset:0 sc1\n"
      "ds_write_b32 v155, v232\n"
      "v_mfma_f32_16x16x32_bf16 v[68:71], a[4:7], a[140:143], v[68:71]\n"
      "ds_write_b32 v155, v236 offset:256\n"
      "s_waitcnt lgkmcnt(2)\n"
      "v_mfma_f32_16x16x32_bf16 v[0:3], a[8:11], a[72:75], v[0:3]\n"
      "ds_write_b32 v156, v228 offset:512\n"
      "ds_write_b32 v155, v234 offset:1024\n"
      "v_mfma_f32_16x16x32_bf16 v[36:39], a[12:15], a[72:75], v[36:39]\n"
      "ds_read_b128 a[108:111], v121 offset:0\n"
      "ds_write_b32 v155, v238 offset:1280\n"
      "v_mfma_f32_16x16x32_bf16 v[40:43], a[12:15], a[76:79], v[40:43]\n"
      "buffer_load_dwordx4 a[160:163], v254, s[12:15], s57 offen offset:1024 sc1\n"
      "ds_write_b32 v156, v230 offset:1536\n"
      "v_mfma_f32_16x16x32_bf16 v[4:7], a[8:11], a[76:79], v[4:7]\n"
      "ds_read_b128 a[112:115], v121 offset:2048\n"
      "ds_read_b32 v130, v156 offset:0\n"
      "v_mfma_f32_16x16x32_bf16 v[8:11], a[8:11], a[80:83], v[8:11]\n"
      "ds_read_b128 a[116:119], v121 offset:4096\n"
      "ds_read_b32 v131, v156 offset:64\n"
      "v_mfma_f32_16x16x32_bf16 v[44:47], a[12:15], a[80:83], v[44:47]\n"
      "ds_read_b128 a[120:123], v121 offset:6144\n"
      "ds_read_b32 v132, v156 offset:128\n"
      "v_mfma_f32_16x16x32_bf16 v[48:51], a[12:15], a[84:87], v[48:51]\n"
      "buffer_load_dwordx4 a[164:167], v254, s[12:15], s57 offen offset:2048 sc1\n"
      "ds_read_b32 v133, v156 offset:192\n"
      "v_mfma_f32_16x16x32_bf16 v[12:15], a[8:11], a[84:87], v[12:15]\n"
      "ds_read_b128 a[124:127], v121 offset:8192\n"
      "ds_read_b32 v134, v156 offset:256\n"
      "v_mfma_f32_16x16x32_bf16 v[16:19], a[8:11], a[88:91], v[16:19]\n"
      "ds_read_b128 a[128:131], v121 offset:10240\n"
      "ds_read_b32 v135, v156 offset:320\n"
      "v_mfma_f32_16x16x32_bf16 v[52:55], a[12:15], a[88:91], v[52:55]\n"
      "ds_read_b128 a[132:135], v121 offset:12288\n"
      "ds_read_b32 v136, v156 offset:384\n"
      "v_mfma_f32_16x16x32_bf16 v[56:59], a[12:15], a[92:95], v[56:59]\n"
      "buffer_load_dwordx4 a[168:171], v254, s[12:15], s57 offen offset:3072 sc1\n"
      "ds_read_b32 v137, v156 offset:448\n"
      "v_mfma_f32_16x16x32_bf16 v[20:23], a[8:11], a[92:95], v[20:23]\n"
      "ds_read_b128 a[136:139], v121 offset:14336\n"
      "ds_read_b32 v138, v156 offset:512\n"
      "v_mfma_f32_16x16x32_bf16 v[24:27], a[8:11], a[96:99], v[24:27]\n"
      "ds_read_b128 a[140:143], v121 offset:16384\n"
      "ds_read_b32 v234, v157 offset:64\n"
      "v_mfma_f32_16x16x32_bf16 v[60:63], a[12:15], a[96:99], v[60:63]\n"
      "ds_read_b32 v235, v157 offset:128\n"
      "ds_read_b32 v236, v157 offset:192\n"
      "v_mfma_f32_16x16x32_bf16 v[64:67], a[12:15], a[100:103], v[64:67]\n"
      "s_add_u32 s57, s57, 0x1000\n"
      "buffer_load_dwordx4 a[172:175], v254, s[12:15], s57 offen offset:0 sc1\n"
      "v_mfma_f32_16x16x32_bf16 v[28:31], a[8:11], a[100:103], v[28:31]\n"
      "ds_read_b32 v237, v157 offset:256\n"
      "ds_read_b32 v158, v164 offset:0\n"
      "v_mfma_f32_16x16x32_bf16 v[32:35], a[8:11], a[104:107], v[32:35]\n"
      "buffer_load_dwordx4 a[176:179], v254, s[12:15], s57 offen offset:1024 sc1\n"
      "ds_read_b32 v160, v164 offset:64\n"
      "v_mfma_f32_16x16x32_bf16 v[68:71], a[12:15], a[104:107], v[68:71]\n"
      "ds_read_b32 v162, v164 offset:128\n"
      "s_waitcnt vmcnt(10) lgkmcnt(7)\n"
      "v_mfma_f32_16x16x32_bf16 v[0:3], a[16:19], a[108:111], v[0:3]\n"
      "ds_read_b32 v164, v164 offset:192\n"
      "ds_read_b32 v159, v165 offset:0\n"
      "v_mfma_f32_16x16x32_bf16 v[36:39], a[20:23], a[108:111], v[36:39]\n"
      "ds_read_b128 a[148:151], v121 offset:18432\n"
      "ds_read_b32 v161, v165 offset:64\n"
      "v_mfma_f32_16x16x32_bf16 v[4:7], a[16:19], a[112:115], v[4:7]\n"
      "buffer_load_dwordx4 a[180:183], v254, s[12:15], s57 offen offset:2048 sc1\n"
      "ds_read_b32 v163, v165 offset:128\n"
      "v_mfma_f32_16x16x32_bf16 v[40:43], a[20:23], a[112:115], v[40:43]\n"
      "ds_read_b128 a[144:147], v120 offset:18432\n"
      "ds_read_b32 v165, v165 offset:192\n"
      "v_mfma_f32_16x16x32_bf16 v[8:11], a[16:19], a[116:119], v[8:11]\n"
      "ds_read_b128 a[76:79], v120 offset:20480\n"
      "v_mfma_f32_16x16x32_bf16 v[44:47], a[20:23], a[116:119], v[44:47]\n"
      "ds_read_b128 a[80:83], v120 offset:22528\n"
      "v_mfma_f32_16x16x32_bf16 v[12:15], a[16:19], a[120:123], v[12:15]\n"
      "buffer_load_dwordx4 a[184:187], v254, s[12:15], s57 offen offset:3072 sc1\n"
      "v_mfma_f32_16x16x32_bf16 v[48:51], a[20:23], a[120:123], v[48:51]\n"
      "ds_read_b128 a[84:87], v120 offset:24576\n"
      "v_mfma_f32_16x16x32_bf16 v[16:19], a[16:19], a[124:127], v[16:19]\n"
      "ds_read_b128 a[88:91], v120 offset:26624\n"
      "s_waitcnt lgkmcnt(4)\n"
      "v_mfma_f32_16x16x32_bf16 v[52:55], a[20:23], a[124:127], v[52:55]\n"
      "ds_read_b128 a[92:95], v120 offset:28672\n"
      "v_mul_f32 v234, 0x3ea52b5c, v234\n"
      "s_waitcnt vmcnt(9)\n"
      "v_mfma_f32_16x16x32_bf16 v[0:3], a[32:35], a[148:151], v[0:3]\n"
      "s_add_u32 s57, s57, 0x1000\n"
      "buffer_load_dwordx4 a[188:191], v254, s[12:15], s57 offen offset:0 sc1\n"
      "s_waitcnt vmcnt(9)\n"
      "v_mfma_f32_16x16x32_bf16 v[36:39], a[36:39], a[148:151], v[36:39]\n"
      "ds_read_b128 a[96:99], v120 offset:30720\n"
      "v_mul_f32 v235, 0x3ea52b5c, v235\n"
      "v_mfma_f32_16x16x32_bf16 v[0:3], a[24:27], a[144:147], v[0:3]\n"
      "ds_read_b128 a[100:103], v120 offset:32768\n"
      "v_mul_f32 v236, 0x3ea52b5c, v236\n"
      "v_mfma_f32_16x16x32_bf16 v[36:39], a[28:31], a[144:147], v[36:39]\n"
      "ds_read_b128 a[104:107], v120 offset:34816\n"
      "v_mul_f32 v237, 0x3ea52b5c, v237\n"
      "v_mfma_f32_16x16x32_bf16 v[20:23], a[16:19], a[128:131], v[20:23]\n"
      "buffer_load_dwordx4 a[192:195], v254, s[12:15], s57 offen offset:1024 sc1\n"
      "v_mul_f32 v150, v234, v158\n"
      "v_mfma_f32_16x16x32_bf16 v[56:59], a[20:23], a[128:131], v[56:59]\n"
      "v_mul_f32 v151, v234, v159\n"
      "v_mul_f32 v152, v235, v160\n"
      "v_mfma_f32_16x16x32_bf16 v[24:27], a[16:19], a[132:135], v[24:27]\n"
      "buffer_load_dwordx4 a[196:199], v254, s[12:15], s57 offen offset:2048 sc1\n"
      "v_mul_f32 v153, v235, v161\n"
      "v_mfma_f32_16x16x32_bf16 v[60:63], a[20:23], a[132:135], v[60:63]\n"
      "v_mul_f32 v154, v236, v162\n"
      "v_mul_f32 v155, v236, v163\n"
      "v_mfma_f32_16x16x32_bf16 v[28:31], a[16:19], a[136:139], v[28:31]\n"
      "buffer_load_dwordx4 a[200:203], v254, s[12:15], s57 offen offset:3072 sc1\n"
      "v_mul_f32 v156, v237, v164\n"
      "v_mfma_f32_16x16x32_bf16 v[64:67], a[20:23], a[136:139], v[64:67]\n"
      "v_mul_f32 v157, v237, v165\n"
      "v_fmac_f32_dpp v4, v0, v131 row_newbcast:0 row_mask:0xf bank_mask:0xf\n"
      "v_mfma_f32_16x16x32_bf16 v[32:35], a[16:19], a[140:143], v[32:35]\n"
      "v_fmac_f32_dpp v5, v1, v131 row_newbcast:0 row_mask:0xf bank_mask:0xf\n"
      "v_fmac_f32_dpp v6, v2, v131 row_newbcast:0 row_mask:0xf bank_mask:0xf\n"
      "v_mfma_f32_16x16x32_bf16 v[68:71], a[20:23], a[140:143], v[68:71]\n"
      "v_fmac_f32_dpp v7, v3, v131 row_newbcast:0 row_mask:0xf bank_mask:0xf\n"
      "s_waitcnt lgkmcnt(0)\n"
      "s_nop 0\n"
      "v_mfma_f32_16x16x32_bf16 v[4:7], a[24:27], a[76:79], v[4:7]\n"
      "v_fmac_f32_dpp v8, v0, v132 row_newbcast:0 row_mask:0xf bank_mask:0xf\n"
      "v_fmac_f32_dpp v9, v1, v132 row_newbcast:0 row_mask:0xf bank_mask:0xf\n"
      "v_mfma_f32_16x16x32_bf16 v[40:43], a[28:31], a[76:79], v[40:43]\n"
      "ds_read_b128 a[112:115], v121 offset:20480\n"
      "v_fmac_f32_dpp v44, v36, v132 row_newbcast:0 row_mask:0xf bank_mask:0xf\n"
      "v_mfma_f32_16x16x32_bf16 v[8:11], a[24:27], a[80:83], v[8:11]\n"
      "s_add_u32 s57, s57, 0x1000\n"
      "buffer_load_dwordx4 a[204:207], v254, s[12:15], s57 offen offset:0 sc1\n"
      "v_mfma_f32_16x16x32_bf16 v[44:47], a[28:31], a[80:83], v[44:47]\n"
      "ds_read_b128 a[116:119], v121 offset:22528\n"
      "v_fmac_f32_dpp v48, v36, v133 row_newbcast:0 row_mask:0xf bank_mask:0xf\n"
      "v_mfma_f32_16x16x32_bf16 v[12:15], a[24:27], a[84:87], v[12:15]\n"
      "ds_read_b128 a[120:123], v121 offset:24576\n"
      "v_fmac_f32_dpp v40, v36, v131 row_newbcast:0 row_mask:0xf bank_mask:0xf\n"
      "v_mfma_f32_16x16x32_bf16 v[48:51], a[28:31], a[84:87], v[48:51]\n"
      "ds_read_b128 a[124:127], v121 offset:26624\n"
      "v_fmac_f32_dpp v41, v37, v131 row_newbcast:0 row_mask:0xf bank_mask:0xf\n"
      "v_mfma_f32_16x16x32_bf16 v[16:19], a[24:27], a[88:91], v[16:19]\n"
      "buffer_load_dwordx4 a[208:211], v254, s[12:15], s57 offen offset:1024 sc1\n"
      "v_fmac_f32_dpp v42, v38, v131 row_newbcast:0 row_mask:0xf bank_mask:0xf\n"
      "v_mfma_f32_16x16x32_bf16 v[52:55], a[28:31], a[88:91], v[52:55]\n"
      "ds_read_b128 a[128:131], v121 offset:28672\n"
      "v_fmac_f32_dpp v43, v39, v131 row_newbcast:0 row_mask:0xf bank_mask:0xf\n"
      "v_mfma_f32_16x16x32_bf16 v[20:23], a[24:27], a[92:95], v[20:23]\n"
      "ds_read_b128 a[132:135], v121 offset:30720\n"
      "v_fmac_f32_dpp v10, v2, v132 row_newbcast:0 row_mask:0xf bank_mask:0xf\n"
      "v_mfma_f32_16x16x32_bf16 v[56:59], a[28:31], a[92:95], v[56:59]\n"
      "ds_read_b128 a[136:139], v121 offset:32768\n"
      "v_fmac_f32_dpp v11, v3, v132 row_newbcast:0 row_mask:0xf bank_mask:0xf\n"
      "v_mfma_f32_16x16x32_bf16 v[24:27], a[24:27], a[96:99], v[24:27]\n"
      "buffer_load_dwordx4 a[212:215], v254, s[12:15], s57 offen offset:2048 sc1\n"
      "v_fmac_f32_dpp v45, v37, v132 row_newbcast:0 row_mask:0xf bank_mask:0xf\n"
      "v_mfma_f32_16x16x32_bf16 v[60:63], a[28:31], a[96:99], v[60:63]\n"
      "ds_read_b128 a[140:143], v121 offset:34816\n"
      "v_fmac_f32_dpp v46, v38, v132 row_newbcast:0 row_mask:0xf bank_mask:0xf\n"
      "v_mfma_f32_16x16x32_bf16 v[28:31], a[24:27], a[100:103], v[28:31]\n"
      "v_fmac_f32_dpp v47, v39, v132 row_newbcast:0 row_mask:0xf bank_mask:0xf\n"
      "v_fmac_f32_dpp v12, v0, v133 row_newbcast:0 row_mask:0xf bank_mask:0xf\n"
      "v_mfma_f32_16x16x32_bf16 v[64:67], a[28:31], a[100:103], v[64:67]\n"
      "v_fmac_f32_dpp v13, v1, v133 row_newbcast:0 row_mask:0xf bank_mask:0xf\n"
      "v_fmac_f32_dpp v14, v2, v133 row_newbcast:0 row_mask:0xf bank_mask:0xf\n"
      "v_mfma_f32_16x16x32_bf16 v[32:35], a[24:27], a[104:107], v[32:35]\n"
      "buffer_load_dwordx4 a[216:219], v254, s[12:15], s57 offen offset:3072 sc1\n"
      "v_fmac_f32_dpp v15, v3, v133 row_newbcast:0 row_mask:0xf bank_mask:0xf\n"
      "v_mfma_f32_16x16x32_bf16 v[68:71], a[28:31], a[104:107], v[68:71]\n"
      "v_fmac_f32_dpp v49, v37, v133 row_newbcast:0 row_mask:0xf bank_mask:0xf\n"
      "s_waitcnt lgkmcnt(0)\n"
      "v_mfma_f32_16x16x32_bf16 v[4:7], a[32:35], a[112:115], v[4:7]\n"
      "v_fmac_f32_dpp v50, v38, v133 row_newbcast:0 row_mask:0xf bank_mask:0xf\n"
      "v_fmac_f32_dpp v51, v39, v133 row_newbcast:0 row_mask:0xf bank_mask:0xf\n"
      "v_mfma_f32_16x16x32_bf16 v[40:43], a[36:39], a[112:115], v[40:43]\n"
      "v_fmac_f32_dpp v16, v0, v134 row_newbcast:0 row_mask:0xf bank_mask:0xf\n"
      "v_fmac_f32_dpp v17, v1, v134 row_newbcast:0 row_mask:0xf bank_mask:0xf\n"
      "v_mfma_f32_16x16x32_bf16 v[8:11], a[32:35], a[116:119], v[8:11]\n"
      "s_add_u32 s57, s57, 0x1000\n"
      "buffer_load_dwordx4 a[220:223], v254, s[12:15], s57 offen offset:0 sc1\n"
      "v_mfma_f32_16x16x32_bf16 v[44:47], a[36:39], a[116:119], v[44:47]\n"
      "v_fmac_f32_dpp v18, v2, v134 row_newbcast:0 row_mask:0xf bank_mask:0xf\n"
      "v_fmac_f32_dpp v19, v3, v134 row_newbcast:0 row_mask:0xf bank_mask:0xf\n"
      "v_mfma_f32_16x16x32_bf16 v[12:15], a[32:35], a[120:123], v[12:15]\n"
      "v_fmac_f32_dpp v52, v36, v134 row_newbcast:0 row_mask:0xf bank_mask:0xf\n"
      "v_fmac_f32_dpp v53, v37, v134 row_newbcast:0 row_mask:0xf bank_mask:0xf\n"
      "v_mfma_f32_16x16x32_bf16 v[48:51], a[36:39], a[120:123], v[48:51]\n"
      "v_fmac_f32_dpp v54, v38, v134 row_newbcast:0 row_mask:0xf bank_mask:0xf\n"
      "v_fmac_f32_dpp v55, v39, v134 row_newbcast:0 row_mask:0xf bank_mask:0xf\n"
      "v_mfma_f32_16x16x32_bf16 v[16:19], a[32:35], a[124:127], v[16:19]\n"
      "buffer_load_dwordx4 a[224:227], v254, s[12:15], s57 offen offset:1024 sc1\n"
      "v_fmac_f32_dpp v20, v0, v135 row_newbcast:0 row_mask:0xf bank_mask:0xf\n"
      "v_mfma_f32_16x16x32_bf16 v[52:55], a[36:39], a[124:127], v[52:55]\n"
      "v_fmac_f32_dpp v56, v36, v135 row_newbcast:0 row_mask:0xf bank_mask:0xf\n"
      "v_fmac_f32_dpp v57, v37, v135 row_newbcast:0 row_mask:0xf bank_mask:0xf\n"
      "v_mfma_f32_16x16x32_bf16 v[20:23], a[32:35], a[128:131], v[20:23]\n"
      "v_fmac_f32_dpp v24, v0, v136 row_newbcast:0 row_mask:0xf bank_mask:0xf\n"
      "v_fmac_f32_dpp v25, v1, v136 row_newbcast:0 row_mask:0xf bank_mask:0xf\n"
      "v_mfma_f32_16x16x32_bf16 v[56:59], a[36:39], a[128:131], v[56:59]\n"
      "v_fmac_f32_dpp v60, v36, v136 row_newbcast:0 row_mask:0xf bank_mask:0xf\n"
      "v_fmac_f32_dpp v61, v37, v136 row_newbcast:0 row_mask:0xf bank_mask:0xf\n"
      "v_mfma_f32_16x16x32_bf16 v[24:27], a[32:35], a[132:135], v[24:27]\n"
      "buffer_load_dwordx4 a[228:231], v254, s[12:15], s57 offen offset:2048 sc1\n"
      "v_fmac_f32_dpp v28, v0, v137 row_newbcast:0 row_mask:0xf bank_mask:0xf\n"
      "v_mfma_f32_16x16x32_bf16 v[60:63], a[36:39], a[132:135], v[60:63]\n"
      "v_fmac_f32_dpp v21, v1, v135 row_newbcast:0 row_mask:0xf bank_mask:0xf\n"
      "v_fmac_f32_dpp v22, v2, v135 row_newbcast:0 row_mask:0xf bank_mask:0xf\n"
      "v_mfma_f32_16x16x32_bf16 v[28:31], a[32:35], a[136:139], v[28:31]\n"
      "v_fmac_f32_dpp v23, v3, v135 row_newbcast:0 row_mask:0xf bank_mask:0xf\n"
      "v_fmac_f32_dpp v58, v38, v135 row_newbcast:0 row_mask:0xf bank_mask:0xf\n"
      "v_mfma_f32_16x16x32_bf16 v[64:67], a[36:39], a[136:139], v[64:67]\n"
      "v_fmac_f32_dpp v59, v39, v135 row_newbcast:0 row_mask:0xf bank_mask:0xf\n"
      "v_fmac_f32_dpp v26, v2, v136 row_newbcast:0 row_mask:0xf bank_mask:0xf\n"
      "v_mfma_f32_16x16x32_bf16 v[32:35], a[32:35], a[140:143], v[32:35]\n"
      "buffer_load_dwordx4 a[232:235], v254, s[12:15], s57 offen offset:3072 sc1\n"
      "v_fmac_f32_dpp v27, v3, v136 row_newbcast:0 row_mask:0xf bank_mask:0xf\n"
      "v_mfma_f32_16x16x32_bf16 v[68:71], a[36:39], a[140:143], v[68:71]\n"
      "v_fmac_f32_dpp v62, v38, v136 row_newbcast:0 row_mask:0xf bank_mask:0xf\n"
      "v_cvt_pk_bf16_f32 v84, v4, v5\n"
      "v_cvt_pk_bf16_f32 v85, v6, v7\n"
      "ds_write_b64 v139, v[84:85] offset:512\n"
      "v_cvt_pk_bf16_f32 v86, v40, v41\n"
      "v_cvt_pk_bf16_f32 v87, v42, v43\n"
      "ds_write_b64 v139, v[86:87] offset:5120\n"
      "v_cvt_pk_bf16_f32 v88, v8, v9\n"
      "v_cvt_pk_bf16_f32 v89, v10, v11\n"
      "ds_write_b64 v139, v[88:89] offset:1024\n"
      "v_cvt_pk_bf16_f32 v90, v44, v45\n"
      "s_add_u32 s57, s57, 0x1000\n"
      "buffer_load_dwordx4 a[236:239], v254, s[12:15], s57 offen offset:0 sc1\n"
      "v_cvt_pk_bf16_f32 v91, v46, v47\n"
      "ds_write_b64 v139, v[90:91] offset:5632\n"
      "v_cvt_pk_bf16_f32 v92, v12, v13\n"
      "v_cvt_pk_bf16_f32 v93, v14, v15\n"
      "ds_write_b64 v139, v[92:93] offset:1536\n"
      "v_cvt_pk_bf16_f32 v94, v48, v49\n"
      "v_cvt_pk_bf16_f32 v95, v50, v51\n"
      "ds_write_b64 v139, v[94:95] offset:6144\n"
      "v_cvt_pk_bf16_f32 v96, v16, v17\n"
      "v_cvt_pk_bf16_f32 v97, v18, v19\n"
      "buffer_load_dwordx4 a[240:243], v254, s[12:15], s57 offen offset:1024 sc1\n"
      "ds_write_b64 v139, v[96:97] offset:2048\n"
      "v_cvt_pk_bf16_f32 v98, v52, v53\n"
      "v_cvt_pk_bf16_f32 v99, v54, v55\n"
      "ds_write_b64 v139, v[98:99] offset:6656\n"
      "v_cvt_pk_bf16_f32 v100, v20, v21\n"
      "v_cvt_pk_bf16_f32 v101, v22, v23\n"
      "ds_write_b64 v139, v[100:101] offset:2560\n"
      "v_cvt_pk_bf16_f32 v102, v56, v57\n"
      "v_cvt_pk_bf16_f32 v103, v58, v59\n"
      "buffer_load_dwordx4 a[244:247], v254, s[12:15], s57 offen offset:2048 sc1\n"
      "ds_write_b64 v139, v[102:103] offset:7168\n"
      "v_fmac_f32_dpp v63, v39, v136 row_newbcast:0 row_mask:0xf bank_mask:0xf\n"
      "v_cvt_pk_bf16_f32 v104, v24, v25\n"
      "v_cvt_pk_bf16_f32 v105, v26, v27\n"
      "ds_write_b64 v139, v[104:105] offset:3072\n"
      "v_cvt_pk_bf16_f32 v106, v60, v61\n"
      "v_cvt_pk_bf16_f32 v107, v62, v63\n"
      "ds_write_b64 v139, v[106:107] offset:7680\n"
      "v_fmac_f32_dpp v29, v1, v137 row_newbcast:0 row_mask:0xf bank_mask:0xf\n"
      "v_fmac_f32_dpp v30, v2, v137 row_newbcast:0 row_mask:0xf bank_mask:0xf\n"
      "buffer_load_dwordx4 a[248:251], v254, s[12:15], s57 offen offset:3072 sc1\n"
      "v_fmac_f32_dpp v31, v3, v137 row_newbcast:0 row_mask:0xf bank_mask:0xf\n"
      "v_fmac_f32_dpp v64, v36, v137 row_newbcast:0 row_mask:0xf bank_mask:0xf\n"
      "v_fmac_f32_dpp v65, v37, v137 row_newbcast:0 row_mask:0xf bank_mask:0xf\n"
      "v_fmac_f32_dpp v66, v38, v137 row_newbcast:0 row_mask:0xf bank_mask:0xf\n"
      "v_fmac_f32_dpp v67, v39, v137 row_newbcast:0 row_mask:0xf bank_mask:0xf\n"
      "v_cvt_pk_bf16_f32 v108, v28, v29\n"
      "v_cvt_pk_bf16_f32 v109, v30, v31\n"
      "ds_write_b64 v139, v[108:109] offset:3584\n"
      "v_cvt_pk_bf16_f32 v110, v64, v65\n"
      "s_add_u32 s57, s57, 0x1000\n"
      "buffer_load_dwordx4 a[252:255], v254, s[12:15], s57 offen offset:0 sc1\n"
      "v_cvt_pk_bf16_f32 v111, v66, v67\n"
      "ds_write_b64 v139, v[110:111] offset:8192\n"
      "v_fmac_f32_dpp v32, v0, v138 row_newbcast:0 row_mask:0xf bank_mask:0xf\n"
      "v_fmac_f32_dpp v33, v1, v138 row_newbcast:0 row_mask:0xf bank_mask:0xf\n"
      "v_fmac_f32_dpp v34, v2, v138 row_newbcast:0 row_mask:0xf bank_mask:0xf\n"
      "v_fmac_f32_dpp v35, v3, v138 row_newbcast:0 row_mask:0xf bank_mask:0xf\n"
      "v_fmac_f32_dpp v68, v36, v138 row_newbcast:0 row_mask:0xf bank_mask:0xf\n"
      "v_fmac_f32_dpp v69, v37, v138 row_newbcast:0 row_mask:0xf bank_mask:0xf\n"
      "v_fmac_f32_dpp v70, v38, v138 row_newbcast:0 row_mask:0xf bank_mask:0xf\n"
      "v_fmac_f32_dpp v71, v39, v138 row_newbcast:0 row_mask:0xf bank_mask:0xf\n"
      "buffer_load_dwordx4 a[0:3], v254, s[12:15], s59 offen offset:0 sc1\n"
      "v_cvt_pk_bf16_f32 v112, v32, v33\n"
      "v_cvt_pk_bf16_f32 v113, v34, v35\n"
      "ds_write_b64 v139, v[112:113] offset:4096\n"
      "v_cvt_pk_bf16_f32 v114, v68, v69\n"
      "v_cvt_pk_bf16_f32 v115, v70, v71\n"
      "ds_write_b64 v139, v[114:115] offset:8704\n"
      "v_fmac_f32_dpp v0, v0, v130 row_newbcast:0 row_mask:0xf bank_mask:0xf\n"
      "v_fmac_f32_dpp v1, v1, v130 row_newbcast:0 row_mask:0xf bank_mask:0xf\n"
      "v_fmac_f32_dpp v2, v2, v130 row_newbcast:0 row_mask:0xf bank_mask:0xf\n"
      "buffer_load_dwordx4 a[4:7], v254, s[12:15], s59 offen offset:1024 sc1\n"
      "v_fmac_f32_dpp v3, v3, v130 row_newbcast:0 row_mask:0xf bank_mask:0xf\n"
      "v_fmac_f32_dpp v36, v36, v130 row_newbcast:0 row_mask:0xf bank_mask:0xf\n"
      "v_fmac_f32_dpp v37, v37, v130 row_newbcast:0 row_mask:0xf bank_mask:0xf\n"
      "v_fmac_f32_dpp v38, v38, v130 row_newbcast:0 row_mask:0xf bank_mask:0xf\n"
      "v_fmac_f32_dpp v39, v39, v130 row_newbcast:0 row_mask:0xf bank_mask:0xf\n"
      "v_cvt_pk_bf16_f32 v80, v0, v1\n"
      "v_cvt_pk_bf16_f32 v81, v2, v3\n"
      "ds_write_b64 v139, v[80:81] offset:0\n"
      "v_cvt_pk_bf16_f32 v82, v36, v37\n"
      "v_cvt_pk_bf16_f32 v83, v38, v39\n"
      "buffer_load_dwordx4 a[8:11], v254, s[12:15], s59 offen offset:2048 sc1\n"
      "ds_write_b64 v139, v[82:83] offset:4608\n"
      "s_waitcnt lgkmcnt(0)\n"
      "s_barrier\n"
      "ds_read_b64 v[212:213], v140 offset:4608\n"
      "ds_read_b64 v[214:215], v142 offset:13952\n"
      "ds_read_b64 v[216:217], v140 offset:5120\n"
      "ds_read_b64 v[218:219], v142 offset:14464\n"
      "ds_read_b64 v[196:197], v140 offset:512\n"
      "ds_read_b64 v[198:199], v141 offset:14336\n"
      "ds_read_b64 v[220:221], v140 offset:5632\n"
      "buffer_load_dwordx4 a[12:15], v254, s[12:15], s59 offen offset:3072 sc1\n"
      "ds_read_b64 v[222:223], v142 offset:14976\n"
      "ds_read_b64 v[200:201], v140 offset:1024\n"
      "ds_read_b64 v[202:203], v141 offset:14848\n"
      "ds_read_b64 v[224:225], v140 offset:6144\n"
      "ds_read_b64 v[226:227], v142 offset:15488\n"
      "ds_read_b64 v[204:205], v140 offset:1536\n"
      "ds_read_b64 v[206:207], v141 offset:15360\n"
      "ds_read_b64 v[228:229], v140 offset:6656\n"
      "ds_read_b64 v[230:231], v142 offset:16000\n"
      "ds_read_b64 v[208:209], v140 offset:2048\n"
      "s_add_u32 s59, s59, 0x1000\n"
      "buffer_load_dwordx4 a[16:19], v254, s[12:15], s59 offen offset:0 sc1\n"
      "ds_read_b64 v[210:211], v141 offset:15872\n"
      "s_waitcnt lgkmcnt(12)\n"
      "v_mfma_f32_16x16x32_bf16 v[80:83], v[212:215], v[196:199], v[174:177]\n"
      "v_mfma_f32_16x16x32_bf16 v[84:87], v[216:219], v[196:199], v[170:173]\n"
      "s_waitcnt lgkmcnt(8)\n"
      "v_mfma_f32_16x16x32_bf16 v[88:91], v[216:219], v[200:203], v[166:169]\n"
      "v_mfma_f32_16x16x32_bf16 v[92:95], v[220:223], v[200:203], v[170:173]\n"
      "s_waitcnt lgkmcnt(4)\n"
      "v_mfma_f32_16x16x32_bf16 v[96:99], v[220:223], v[204:207], v[166:169]\n"
      "v_mfma_f32_16x16x32_bf16 v[100:103], v[224:227], v[204:207], v[170:173]\n"
      "s_waitcnt lgkmcnt(0)\n"
      "v_mfma_f32_16x16x32_bf16 v[104:107], v[224:227], v[208:211], v[166:169]\n"
      "v_mfma_f32_16x16x32_bf16 v[108:111], v[228:231], v[208:211], v[170:173]\n"
      "buffer_load_dwordx4 a[20:23], v254, s[12:15], s59 offen offset:1024 sc1\n"
      "ds_read_b64_tr_b16 v[232:233], v143 offset:9216\n"
      "ds_read_b64_tr_b16 v[242:243], v144 offset:14080\n"
      "ds_read_b64_tr_b16 v[234:235], v143 offset:9728\n"
      "ds_read_b64_tr_b16 v[244:245], v144 offset:14592\n"
      "v_max3_f32 v0, v80, v81, v84\n"
      "v_max_f32 v0, v0, v85\n"
      "v_max3_f32 v1, v82, v83, v86\n"
      "v_max_f32 v1, v1, v87\n"
      "v_max3_f32 v2, v88, v89, v92\n"
      "v_max_f32 v2, v2, v93\n"
      "v_max3_f32 v3, v90, v91, v94\n"
      "v_max_f32 v3, v3, v95\n"
      "v_max3_f32 v4, v96, v97, v100\n"
      "v_max_f32 v4, v4, v101\n"
      "v_max3_f32 v5, v98, v99, v102\n"
      "v_max_f32 v5, v5, v103\n"
      "v_max3_f32 v6, v104, v105, v108\n"
      "v_max_f32 v6, v6, v109\n"
      "v_max3_f32 v7, v106, v107, v110\n"
      "v_max_f32 v7, v7, v111\n"
      "buffer_load_dwordx4 a[24:27], v254, s[12:15], s59 offen offset:2048 sc1\n"
      "v_mul_f32 v0, v0, v150\n"
      "v_mul_f32 v1, v1, v151\n"
      "v_max_f32 v8, v0, v1\n"
      "v_mul_f32 v2, v2, v152\n"
      "v_mul_f32 v3, v3, v153\n"
      "v_max_f32 v9, v2, v3\n"
      "v_mul_f32 v4, v4, v154\n"
      "v_mul_f32 v5, v5, v155\n"
      "v_max_f32 v10, v4, v5\n"
      "v_mul_f32 v6, v6, v156\n"
      "v_mul_f32 v7, v7, v157\n"
      "v_max_f32 v11, v6, v7\n"
      "buffer_load_dwordx4 a[28:31], v254, s[12:15], s59 offen offset:3072 sc1\n"
      "ds_read_b64_tr_b16 v[236:237], v143 offset:10240\n"
      "v_permlane32_swap_b32 v8, v9\n"
      "v_permlane32_swap_b32 v10, v11\n"
      "v_max_f32 v12, v8, v9\n"
      "v_max_f32 v13, v10, v11\n"
      "ds_read_b64_tr_b16 v[246:247], v144 offset:15104\n"
      "ds_read_b64_tr_b16 v[238:239], v143 offset:10752\n"
      "v_permlane16_swap_b32 v12, v13\n"
      "v_max_f32 v8, v12, v13\n"
      "v_mov_b32 v10, v8\n"
      "ds_read_b64_tr_b16 v[248:249], v144 offset:15616\n"
      "ds_read_b64_tr_b16 v[240:241], v143 offset:11264\n"
      "v_permlane16_swap_b32 v8, v10\n"
      "v_mov_b32 v9, v8\n"
      "v_mov_b32 v11, v10\n"
      "ds_read_b64_tr_b16 v[250:251], v144 offset:16128\n"
      "v_permlane32_swap_b32 v8, v9\n"
      "v_permlane32_swap_b32 v10, v11\n"
      "s_add_u32 s59, s59, 0x1000\n"
      "buffer_load_dwordx4 a[32:35], v254, s[12:15], s59 offen offset:0 sc1\n"
      "v_sub_f32 v0, v0, v8\n"
      "v_sub_f32 v1, v1, v8\n"
      "v_sub_f32 v2, v2, v9\n"
      "v_sub_f32 v3, v3, v9\n"
      "v_sub_f32 v4, v4, v10\n"
      "v_sub_f32 v5, v5, v10\n"
      "v_sub_f32 v6, v6, v11\n"
      "v_sub_f32 v7, v7, v11\n"
      "v_exp_f32 v0, v0\n"
      "v_exp_f32 v1, v1\n"
      "v_exp_f32 v2, v2\n"
      "v_exp_f32 v3, v3\n"
      "v_exp_f32 v4, v4\n"
      "v_exp_f32 v5, v5\n"
      "v_exp_f32 v6, v6\n"
      "v_exp_f32 v7, v7\n"
      "buffer_load_dwordx4 a[36:39], v254, s[12:15], s59 offen offset:1024 sc1\n"
      "v_add_f32 v8, v0, v1\n"
      "v_add_f32 v9, v2, v3\n"
      "v_add_f32 v10, v4, v5\n"
      "v_add_f32 v11, v6, v7\n"
      "buffer_load_dwordx4 a[40:43], v254, s[12:15], s59 offen offset:2048 sc1\n"
      "v_mul_f32 v0, v0, v158\n"
      "v_permlane32_swap_b32 v8, v9\n"
      "v_permlane32_swap_b32 v10, v11\n"
      "v_add_f32 v12, v8, v9\n"
      "v_add_f32 v13, v10, v11\n"
      "v_mul_f32 v1, v1, v159\n"
      "v_mul_f32 v2, v2, v160\n"
      "v_permlane16_swap_b32 v12, v13\n"
      "v_add_f32 v8, v12, v13\n"
      "v_rcp_f32 v8, v8\n"
      "v_mul_f32 v3, v3, v161\n"
      "v_mov_b32 v10, v8\n"
      "v_mul_f32 v4, v4, v162\n"
      "v_mul_f32 v5, v5, v163\n"
      "v_permlane16_swap_b32 v8, v10\n"
      "v_mov_b32 v9, v8\n"
      "v_mov_b32 v11, v10\n"
      "v_mul_f32 v6, v6, v164\n"
      "v_mul_f32 v7, v7, v165\n"
      "v_permlane32_swap_b32 v8, v9\n"
      "v_permlane32_swap_b32 v10, v11\n"
      "buffer_load_dwordx4 a[44:47], v254, s[12:15], s59 offen offset:3072 sc1\n"
      "v_mul_f32 v0, v0, v8\n"
      "v_mul_f32 v1, v1, v8\n"
      "v_mul_f32 v2, v2, v9\n"
      "v_mul_f32 v3, v3, v9\n"
      "v_mul_f32 v4, v4, v10\n"
      "v_mul_f32 v5, v5, v10\n"
      "v_mul_f32 v6, v6, v11\n"
      "v_mul_f32 v7, v7, v11\n"
      "v_cvt_pk_bf16_f32 v0, v0, v0\n"
      "v_cvt_pk_bf16_f32 v1, v1, v1\n"
      "v_cvt_pk_bf16_f32 v2, v2, v2\n"
      "v_cvt_pk_bf16_f32 v3, v3, v3\n"
      ".Lk_pad3:\n"
      "s_cbranch_execz .Lk_hopret\n"
      "v_cvt_pk_bf16_f32 v4, v4, v4\n"
      "v_cvt_pk_bf16_f32 v5, v5, v5\n"
      "v_cvt_pk_bf16_f32 v6, v6, v6\n"
      "v_cvt_pk_bf16_f32 v7, v7, v7\n"
      "v_and_b32 v20, v0, v178\n"
      "v_and_b32 v21, v1, v179\n"
      "v_and_b32 v22, v0, v180\n"
      "v_and_b32 v23, v1, v181\n"
      "v_and_b32 v24, v2, v178\n"
      "v_and_b32 v25, v3, v179\n"
      "v_and_b32 v26, v2, v180\n"
      "v_and_b32 v27, v3, v181\n"
      "v_and_b32 v28, v4, v178\n"
      "v_and_b32 v29, v5, v179\n"
      "v_and_b32 v30, v4, v180\n"
      "v_and_b32 v31, v5, v181\n"
      "v_and_b32 v32, v6, v178\n"
      "v_and_b32 v33, v7, v179\n"
      "v_and_b32 v34, v6, v180\n"
      "v_and_b32 v35, v7, v181\n"
      "s_waitcnt lgkmcnt(7)\n"
      "v_mfma_f32_16x16x32_bf16 v[80:83], v[232:235], v[20:23], 0\n"
      "s_waitcnt lgkmcnt(5)\n"
      "v_mfma_f32_16x16x32_bf16 v[84:87], v[234:237], v[24:27], 0\n"
      "v_and_b32 v116, 1, v253\n"
      "s_waitcnt lgkmcnt(3)\n"
      "v_mfma_f32_16x16x32_bf16 v[88:91], v[236:239], v[28:31], 0\n"
      "v_lshrrev_b32 v117, 1, v253\n"
      "s_waitcnt lgkmcnt(1)\n"
      "v_mfma_f32_16x16x32_bf16 v[92:95], v[238:241], v[32:35], 0\n"
      "v_mul_u32_u24 v116, 0x5000, v116\n"
      "v_mfma_f32_16x16x32_bf16 v[96:99], v[242:245], v[20:23], 0\n"
      "v_lshl_add_u32 v116, v117, 8, v116\n"
      "v_mfma_f32_16x16x32_bf16 v[100:103], v[244:247], v[24:27], 0\n"
      "v_lshl_add_u32 v116, v252, 4, v116\n"
      "v_mfma_f32_16x16x32_bf16 v[104:107], v[246:249], v[28:31], 0\n"
      "v_mul_u32_u24 v117, 0x5000, v253\n"
      "s_waitcnt lgkmcnt(0)\n"
      "v_mfma_f32_16x16x32_bf16 v[108:111], v[248:251], v[32:35], 0\n"
      "v_lshl_add_u32 v117, v252, 3, v117\n"
      "v_cvt_pk_bf16_f32 v40, v80, v81\n"
      "v_cvt_pk_bf16_f32 v41, v82, v83\n"
      "v_cvt_pk_bf16_f32 v42, v84, v85\n"
      "v_cvt_pk_bf16_f32 v43, v86, v87\n"
      "v_cvt_pk_bf16_f32 v44, v88, v89\n"
      "v_cvt_pk_bf16_f32 v45, v90, v91\n"
      "v_cvt_pk_bf16_f32 v46, v92, v93\n"
      "v_cvt_pk_bf16_f32 v47, v94, v95\n"
      "v_permlane16_swap_b32 v40, v42\n"
      "v_permlane16_swap_b32 v41, v43\n"
      "v_permlane16_swap_b32 v44, v46\n"
      "v_permlane16_swap_b32 v45, v47\n"
      "buffer_store_dwordx4 v[40:43], v116, s[12:15], s92 offen sc1\n"
      "buffer_store_dwordx4 v[44:47], v116, s[12:15], s93 offen sc1\n"
      "v_cvt_pk_bf16_f32 v48, v96, v97\n"
      "v_cvt_pk_bf16_f32 v49, v98, v99\n"
      "s_mov_b32 exec_lo, 0xffff0000\n"
      "s_mov_b32 exec_hi, 0x0\n"
      "v_cvt_pk_bf16_f32 v48, v100, v101\n"
      "v_cvt_pk_bf16_f32 v49, v102, v103\n"
      "s_mov_b32 exec_hi, 0xffff\n"
      "s_mov_b32 exec_lo, 0x0\n"
      "v_cvt_pk_bf16_f32 v48, v104, v105\n"
      "v_cvt_pk_bf16_f32 v49, v106, v107\n"
      "s_mov_b32 exec_hi, 0xffff0000\n"
      "s_mov_b32 exec_lo, 0x0\n"
      "v_cvt_pk_bf16_f32 v48, v108, v109\n"
      "v_cvt_pk_bf16_f32 v49, v110, v111\n"
      "s_mov_b64 exec, -1\n"
      "buffer_store_dwordx2 v[48:49], v117, s[12:15], s94 offen sc1\n"
      "s_cmp_eq_u32 s3, 3\n"
      "s_cselect_b32 s45, 0x40000000, 0\n"
      "v_add_u32 v251, s45, v254\n"
      "s_cmp_lt_u32 s35, 12\n"
      "s_cselect_b32 s45, 0, 0x40000000\n"
      "v_add_u32 v250, s45, v254\n"
      "s_add_u32 s81, s58, 655360\n"
      "s_add_u32 m0, s79, 61440\n"
      "s_nop 0\n"
      "buffer_load_dwordx4 v254, s[12:15], s81 offen offset:0 sc1 lds\n"
      "buffer_load_dwordx4 v254, s[12:15], s81 offen offset:1024 sc1 lds\n"
      "buffer_load_dwordx4 v254, s[12:15], s81 offen offset:2048 sc1 lds\n"
      "buffer_load_dwordx4 v254, s[12:15], s81 offen offset:3072 sc1 lds\n"
      "v_lshlrev_b32 v210, 16, v182\n"
      "v_and_b32 v211, 0xffff0000, v182\n"
      "v_lshlrev_b32 v212, 16, v183\n"
      "v_and_b32 v213, 0xffff0000, v183\n"
      "v_lshlrev_b32 v214, 16, v184\n"
      "v_and_b32 v215, 0xffff0000, v184\n"
      "v_lshlrev_b32 v216, 16, v185\n"
      "v_and_b32 v217, 0xffff0000, v185\n"
      "v_lshlrev_b32 v218, 16, v186\n"
      "v_and_b32 v219, 0xffff0000, v186\n"
      "v_lshlrev_b32 v220, 16, v187\n"
      "v_and_b32 v221, 0xffff0000, v187\n"
      "v_lshlrev_b32 v222, 16, v188\n"
      "v_and_b32 v223, 0xffff0000, v188\n"
      "v_lshlrev_b32 v224, 16, v189\n"
      "v_and_b32 v225, 0xffff0000, v189\n"
      "v_lshlrev_b32 v226, 16, v190\n"
      "v_and_b32 v227, 0xffff0000, v190\n"
      "v_lshlrev_b32 v228, 16, v191\n"
      "v_and_b32 v229, 0xffff0000, v191\n"
      "s_add_u32 s44, s3, 1\n"
      "s_and_b32 s44, s44, 3\n"
      "s_mul_i32 s44, s44, 15360\n"
      "s_add_u32 s44, s44, 102400\n"
      "v_add_u32 v202, s44, v254\n"
      "s_add_u32 s44, s3, 2\n"
      "s_and_b32 s44, s44, 3\n"
      "s_mul_i32 s44, s44, 15360\n"
      "s_add_u32 s44, s44, 107520\n"
      "v_add_u32 v203, s44, v254\n"
      "s_add_u32 s44, s3, 3\n"
      "s_and_b32 s44, s44, 3\n"
      "s_mul_i32 s44, s44, 15360\n"
      "s_add_u32 s44, s44, 112640\n"
      "v_add_u32 v204, s44, v254\n"
      "s_mul_i32 s44, s3, 15360\n"
      "s_add_u32 s44, s44, 102400\n"
      "v_add_u32 v205, s44, v254\n"
      "v_lshrrev_b32 v206, 1, v254\n"
      "v_add_u32 v206, 4096, v206\n"
      "s_lshl2_add_u32 s44, s37, s3\n"
      "s_mul_i32 s92, s44, 20480\n"
      "s_mul_i32 s46, s36, 2560\n"
      "s_add_u32 s92, s92, s46\n"
      "s_add_u32 s92, s92, 0xb10000\n"
      "s_lshl3_add_u32 s44, s44, s36\n"
      "s_lshl_b32 s44, s44, 7\n"
      "s_add_u32 s93, s44, 0xd90000\n"
      "v_lshlrev_b32 v196, 7, v253\n"
      "v_lshl_add_u32 v196, v252, 4, v196\n"
      "v_lshlrev_b32 v197, 3, v252\n"
      "v_lshl_add_u32 v197, v253, 1, v197\n"
      "v_mov_b32 v198, 0x3f803f80\n"
      "s_lshl2_add_u32 s44, s34, s3\n"
      "s_lshl_b32 s44, s44, 7\n"
      "s_add_u32 s45, s44, 0x98000\n"
      "v_lshlrev_b32 v2, 1, v254\n"
      "s_lshl_b32 s43, s38, 5\n"
      "s_add_u32 s43, s43, 0x98000\n"
      "v_mov_b32 v4, s40\n"
      "v_mov_b32 v5, s41\n"
      "v_mov_b32 v6, s40\n"
      "v_mov_b32 v7, s41\n"
      "v_add_u32 v8, s42, v254\n"
      "s_mov_b64 exec, 0x3\n"
      "s_waitcnt vmcnt(4)\n"
      "s_barrier\n"
      "buffer_store_dwordx4 v[4:7], v8, s[12:15], s43 offen sc1\n"
      "s_mov_b64 exec, -1\n"
      "s_add_u32 s81, s58, 659456\n"
      "s_add_u32 m0, s79, 65536\n"
      "s_nop 0\n"
      "buffer_load_dwordx4 v254, s[12:15], s81 offen offset:0 sc1 lds\n"
      "s_add_u32 s81, s58, 675840\n"
      "s_add_u32 m0, s79, 81920\n"
      "s_nop 0\n"
      "buffer_load_dwordx4 v254, s[12:15], s81 offen offset:0 sc1 lds\n"
      "buffer_load_dwordx4 v254, s[12:15], s81 offen offset:1024 sc1 lds\n"
      "buffer_load_dwordx4 v254, s[12:15], s81 offen offset:2048 sc1 lds\n"
      "buffer_load_dwordx4 v254, s[12:15], s81 offen offset:3072 sc1 lds\n"
      "s_add_u32 s81, s58, 679936\n"
      "s_add_u32 m0, s79, 86016\n"
      "s_nop 0\n"
      "buffer_load_dwordx4 v254, s[12:15], s81 offen offset:0 sc1 lds\n"
      "s_add_u32 s81, s58, 0\n"
      "s_add_u32 m0, s79, 0\n"
      "s_nop 0\n"
      "buffer_load_dwordx4 v254, s[12:15], s81 offen offset:0 sc1 lds\n"
      "buffer_load_dwordx4 v254, s[12:15], s81 offen offset:1024 sc1 lds\n"
      "buffer_load_dwordx4 v254, s[12:15], s81 offen offset:2048 sc1 lds\n"
      "buffer_load_dwordx4 v254, s[12:15], s81 offen offset:3072 sc1 lds\n"
      "s_add_u32 s81, s58, 4096\n"
      "s_add_u32 m0, s79, 4096\n"
      "s_nop 0\n"
      "buffer_load_dwordx4 v254, s[12:15], s81 offen offset:0 sc1 lds\n"
      "s_add_u32 s81, s58, 20480\n"
      "s_add_u32 m0, s79, 20480\n"
      "s_nop 0\n"
      "buffer_load_dwordx4 v254, s[12:15], s81 offen offset:0 sc1 lds\n"
      "buffer_load_dwordx4 v254, s[12:15], s81 offen offset:1024 sc1 lds\n"
      "buffer_load_dwordx4 v254, s[12:15], s81 offen offset:2048 sc1 lds\n"
      "s_mov_b64 exec, 0xf\n"
      "buffer_load_dwordx2 v[0:1], v2, s[12:15], s45 offen sc1\n"
      "s_mov_b64 exec, -1\n"
      "buffer_load_dwordx4 v254, s[12:15], s81 offen offset:3072 sc1 lds\n"
      "s_add_u32 s81, s58, 24576\n"
      "s_add_u32 m0, s79, 24576\n"
      "s_nop 0\n"
      "buffer_load_dwordx4 v254, s[12:15], s81 offen offset:0 sc1 lds\n"
      "s_add_u32 s81, s80, 0\n"
      "s_add_u32 m0, s79, 40960\n"
      "s_nop 0\n"
      "buffer_load_dwordx4 v250, s[12:15], s81 offen offset:0 sc1 lds\n"
      "buffer_load_dwordx4 v250, s[12:15], s81 offen offset:1024 sc1 lds\n"
      "buffer_load_dwordx4 v250, s[12:15], s81 offen offset:2048 sc1 lds\n"
      "s_waitcnt vmcnt(5)\n"
      "v_cmp_ne_u64 vcc, s[40:41], v[0:1]\n"
      "s_and_b32 vcc_lo, vcc_lo, 0xf\n"
      "s_cbranch_scc0 .Lk_ok_6\n"
      "s_mov_b64 exec, 0xf\n"
      ".Lk_poll_7:\n"
      "s_sleep 1\n"
      "buffer_load_dwordx2 v[0:1], v2, s[12:15], s45 offen sc1\n"
      "s_waitcnt vmcnt(0)\n"
      "v_cmp_ne_u64 vcc, s[40:41], v[0:1]\n"
      "s_cbranch_vccz .Lk_polld_8\n"
      "s_cmp_eq_u32 s39, 0\n"
      "s_cbranch_scc1 .Lk_polld_8\n"
      "s_sub_u32 s39, s39, 1\n"
      "s_branch .Lk_poll_7\n"
      ".Lk_polld_8:\n"
      "s_mov_b64 exec, -1\n"
      ".Lk_ok_6:\n"
      "buffer_load_dwordx4 v[0:3], v254, s[12:15], s64 offen offset:0 sc1\n"
      "buffer_load_dwordx4 v[4:7], v254, s[12:15], s64 offen offset:1024 sc1\n"
      "buffer_load_dwordx4 v[8:11], v254, s[12:15], s64 offen offset:2048 sc1\n"
      "buffer_load_dwordx4 v[12:15], v254, s[12:15], s64 offen offset:3072 sc1\n"
      "buffer_load_dwordx2 v[16:17], v206, s[12:15], s64 offen sc1\n"
      "buffer_load_dwordx2 v[18:19], v206, s[12:15], s64 offen offset:512 sc1\n"
      "buffer_load_dwordx4 v[20:23], v254, s[12:15], s65 offen offset:0 sc1\n"
      "buffer_load_dwordx4 v[24:27], v254, s[12:15], s65 offen offset:1024 sc1\n"
      "buffer_load_dwordx4 v[28:31], v254, s[12:15], s65 offen offset:2048 sc1\n"
      "s_waitcnt vmcnt(8)\n"
      "v_mfma_f32_16x16x32_bf16 v[80:83], a[156:159], v[0:3], 0\n"
      "v_mfma_f32_16x16x32_bf16 v[84:87], a[160:163], v[0:3], 0\n"
      "v_mfma_f32_16x16x32_bf16 v[88:91], a[164:167], v[0:3], 0\n"
      "v_mfma_f32_16x16x32_bf16 v[92:95], a[168:171], v[0:3], 0\n"
      "v_mfma_f32_16x16x32_bf16 v[96:99], a[172:175], v[0:3], 0\n"
      "buffer_load_dwordx4 v[32:35], v254, s[12:15], s65 offen offset:3072 sc1\n"
      "s_waitcnt vmcnt(8)\n"
      "v_mfma_f32_16x16x32_bf16 v[80:83], a[176:179], v[4:7], v[80:83]\n"
      "v_mfma_f32_16x16x32_bf16 v[84:87], a[180:183], v[4:7], v[84:87]\n"
      "v_mfma_f32_16x16x32_bf16 v[88:91], a[184:187], v[4:7], v[88:91]\n"
      "v_mfma_f32_16x16x32_bf16 v[92:95], a[188:191], v[4:7], v[92:95]\n"
      "v_mfma_f32_16x16x32_bf16 v[96:99], a[192:195], v[4:7], v[96:99]\n"
      "buffer_load_dwordx2 v[36:37], v206, s[12:15], s65 offen sc1\n"
      "buffer_load_dwordx2 v[38:39], v206, s[12:15], s65 offen offset:512 sc1\n"
      "s_waitcnt vmcnt(9)\n"
      "v_mfma_f32_16x16x32_bf16 v[80:83], a[196:199], v[8:11], v[80:83]\n"
      "v_mfma_f32_16x16x32_bf16 v[84:87], a[200:203], v[8:11], v[84:87]\n"
      "v_mfma_f32_16x16x32_bf16 v[88:91], a[204:207], v[8:11], v[88:91]\n"
      "v_mfma_f32_16x16x32_bf16 v[92:95], a[208:211], v[8:11], v[92:95]\n"
      "v_mfma_f32_16x16x32_bf16 v[96:99], a[212:215], v[8:11], v[96:99]\n"
      "buffer_load_dwordx4 v[40:43], v254, s[12:15], s66 offen offset:0 sc1\n"
      "s_waitcnt vmcnt(9)\n"
      "v_mfma_f32_16x16x32_bf16 v[80:83], a[216:219], v[12:15], v[80:83]\n"
      "v_mfma_f32_16x16x32_bf16 v[84:87], a[220:223], v[12:15], v[84:87]\n"
      "v_mfma_f32_16x16x32_bf16 v[88:91], a[224:227], v[12:15], v[88:91]\n"
      "v_mfma_f32_16x16x32_bf16 v[92:95], a[228:231], v[12:15], v[92:95]\n"
      "v_mfma_f32_16x16x32_bf16 v[96:99], a[232:235], v[12:15], v[96:99]\n"
      "buffer_load_dwordx4 v[44:47], v254, s[12:15], s66 offen offset:1024 sc1\n"
      "s_waitcnt vmcnt(8)\n"
      "v_mfma_f32_16x16x32_bf16 v[80:83], a[236:239], v[16:19], v[80:83]\n"
      "v_mfma_f32_16x16x32_bf16 v[84:87], a[240:243], v[16:19], v[84:87]\n"
      "v_mfma_f32_16x16x32_bf16 v[88:91], a[244:247], v[16:19], v[88:91]\n"
      "v_mfma_f32_16x16x32_bf16 v[92:95], a[248:251], v[16:19], v[92:95]\n"
      "v_mfma_f32_16x16x32_bf16 v[96:99], a[252:255], v[16:19], v[96:99]\n"
      "buffer_load_dwordx4 v[48:51], v254, s[12:15], s66 offen offset:2048 sc1\n"
      "s_waitcnt vmcnt(8)\n"
      "v_mfma_f32_16x16x32_bf16 v[100:103], a[156:159], v[20:23], 0\n"
      "v_mfma_f32_16x16x32_bf16 v[104:107], a[160:163], v[20:23], 0\n"
      "v_mfma_f32_16x16x32_bf16 v[108:111], a[164:167], v[20:23], 0\n"
      "ds_write_b128 v202, v[80:83] offset:0\n"
      "v_mfma_f32_16x16x32_bf16 v[112:115], a[168:171], v[20:23], 0\n"
      "v_mfma_f32_16x16x32_bf16 v[116:119], a[172:175], v[20:23], 0\n"
      "buffer_load_dwordx4 v[52:55], v254, s[12:15], s66 offen offset:3072 sc1\n"
      "s_waitcnt vmcnt(8)\n"
      "v_mfma_f32_16x16x32_bf16 v[100:103], a[176:179], v[24:27], v[100:103]\n"
      "ds_write_b128 v202, v[84:87] offset:1024\n"
      "v_mfma_f32_16x16x32_bf16 v[104:107], a[180:183], v[24:27], v[104:107]\n"
      "v_mfma_f32_16x16x32_bf16 v[108:111], a[184:187], v[24:27], v[108:111]\n"
      "v_mfma_f32_16x16x32_bf16 v[112:115], a[188:191], v[24:27], v[112:115]\n"
      "ds_write_b128 v202, v[88:91] offset:2048\n"
      "v_mfma_f32_16x16x32_bf16 v[116:119], a[192:195], v[24:27], v[116:119]\n"
      "buffer_load_dwordx2 v[56:57], v206, s[12:15], s66 offen sc1\n"
      "buffer_load_dwordx2 v[58:59], v206, s[12:15], s66 offen offset:512 sc1\n"
      "s_waitcnt vmcnt(9)\n"
      "v_mfma_f32_16x16x32_bf16 v[100:103], a[196:199], v[28:31], v[100:103]\n"
      "v_mfma_f32_16x16x32_bf16 v[104:107], a[200:203], v[28:31], v[104:107]\n"
      "ds_write_b128 v202, v[92:95] offset:3072\n"
      "v_mfma_f32_16x16x32_bf16 v[108:111], a[204:207], v[28:31], v[108:111]\n"
      "v_mfma_f32_16x16x32_bf16 v[112:115], a[208:211], v[28:31], v[112:115]\n"
      "v_mfma_f32_16x16x32_bf16 v[116:119], a[212:215], v[28:31], v[116:119]\n"
      "buffer_load_dwordx4 v[60:63], v254, s[12:15], s67 offen offset:0 sc1\n"
      "ds_write_b128 v202, v[96:99] offset:4096\n"
      "s_waitcnt vmcnt(9)\n"
      "v_mfma_f32_16x16x32_bf16 v[100:103], a[216:219], v[32:35], v[100:103]\n"
      "v_mfma_f32_16x16x32_bf16 v[104:107], a[220:223], v[32:35], v[104:107]\n"
      "v_mfma_f32_16x16x32_bf16 v[108:111], a[224:227], v[32:35], v[108:111]\n"
      "v_mfma_f32_16x16x32_bf16 v[112:115], a[228:231], v[32:35], v[112:115]\n"
      "v_mfma_f32_16x16x32_bf16 v[116:119], a[232:235], v[32:35], v[116:119]\n"
      "buffer_load_dwordx4 v[64:67], v254, s[12:15], s67 offen offset:1024 sc1\n"
      "s_waitcnt vmcnt(8)\n"
      "v_mfma_f32_16x16x32_bf16 v[100:103], a[236:239], v[36:39], v[100:103]\n"
      "v_mfma_f32_16x16x32_bf16 v[104:107], a[240:243], v[36:39], v[104:107]\n"
      "v_mfma_f32_16x16x32_bf16 v[108:111], a[244:247], v[36:39], v[108:111]\n"
      "v_mfma_f32_16x16x32_bf16 v[112:115], a[248:251], v[36:39], v[112:115]\n"
      "v_mfma_f32_16x16x32_bf16 v[116:119], a[252:255], v[36:39], v[116:119]\n"
      "buffer_load_dwordx4 v[68:71], v254, s[12:15], s67 offen offset:2048 sc1\n"
      "s_waitcnt vmcnt(8)\n"
      "v_mfma_f32_16x16x32_bf16 v[120:123], a[156:159], v[40:43], 0\n"
      "s_waitcnt lgkmcnt(0)\n"
      "s_barrier\n"
      "v_mfma_f32_16x16x32_bf16 v[124:127], a[160:163], v[40:43], 0\n"
      "ds_read_b128 v[80:83], v205 offset:0\n"
      "ds_read_b128 v[84:87], v205 offset:1024\n"
      "v_mfma_f32_16x16x32_bf16 v[128:131], a[164:167], v[40:43], 0\n"
      "buffer_load_dwordx4 v250, s[12:15], s81 offen offset:3072 sc1 lds\n"
      "ds_write_b128 v203, v[100:103] offset:0\n"
      "v_mfma_f32_16x16x32_bf16 v[132:135], a[168:171], v[40:43], 0\n"
      "ds_read_b128 v[88:91], v205 offset:2048\n"
      "ds_read_b128 v[92:95], v205 offset:3072\n"
      "v_mfma_f32_16x16x32_bf16 v[136:139], a[172:175], v[40:43], 0\n"
      "buffer_load_dwordx4 v[72:75], v254, s[12:15], s67 offen offset:3072 sc1\n"
      "s_waitcnt vmcnt(9)\n"
      "v_mfma_f32_16x16x32_bf16 v[120:123], a[176:179], v[44:47], v[120:123]\n"
      "ds_write_b128 v203, v[104:107] offset:1024\n"
      "ds_read_b128 v[96:99], v205 offset:4096\n"
      "v_mfma_f32_16x16x32_bf16 v[124:127], a[180:183], v[44:47], v[124:127]\n"
      "v_mfma_f32_16x16x32_bf16 v[128:131], a[184:187], v[44:47], v[128:131]\n"
      "s_add_u32 s81, s80, 4096\n"
      "s_add_u32 m0, s79, 45056\n"
      "s_nop 0\n"
      "buffer_load_dwordx4 v250, s[12:15], s81 offen offset:0 sc1 lds\n"
      "v_mfma_f32_16x16x32_bf16 v[132:135], a[188:191], v[44:47], v[132:135]\n"
      "ds_write_b128 v203, v[108:111] offset:2048\n"
      "v_mfma_f32_16x16x32_bf16 v[136:139], a[192:195], v[44:47], v[136:139]\n"
      "buffer_load_dwordx2 v[76:77], v206, s[12:15], s67 offen sc1\n"
      "buffer_load_dwordx2 v[78:79], v206, s[12:15], s67 offen offset:512 sc1\n"
      "s_waitcnt vmcnt(11)\n"
      "v_mfma_f32_16x16x32_bf16 v[120:123], a[196:199], v[48:51], v[120:123]\n"
      "v_mfma_f32_16x16x32_bf16 v[124:127], a[200:203], v[48:51], v[124:127]\n"
      "s_add_u32 s59, s59, 0x1000\n"
      "buffer_load_dwordx4 a[48:51], v254, s[12:15], s59 offen offset:0 sc1\n"
      "ds_write_b128 v203, v[112:115] offset:3072\n"
      "v_mfma_f32_16x16x32_bf16 v[128:131], a[204:207], v[48:51], v[128:131]\n"
      "v_mfma_f32_16x16x32_bf16 v[132:135], a[208:211], v[48:51], v[132:135]\n"
      "s_waitcnt lgkmcnt(2)\n"
      "v_add_f32 v210, v210, v80\n"
      "v_mfma_f32_16x16x32_bf16 v[136:139], a[212:215], v[48:51], v[136:139]\n"
      "ds_write_b128 v203, v[116:119] offset:4096\n"
      "s_waitcnt vmcnt(11)\n"
      "v_mfma_f32_16x16x32_bf16 v[120:123], a[216:219], v[52:55], v[120:123]\n"
      "v_add_f32 v211, v211, v81\n"
      "v_add_f32 v212, v212, v82\n"
      "v_mfma_f32_16x16x32_bf16 v[124:127], a[220:223], v[52:55], v[124:127]\n"
      "buffer_load_dwordx4 a[52:55], v254, s[12:15], s59 offen offset:1024 sc1\n"
      "v_add_f32 v213, v213, v83\n"
      "v_mfma_f32_16x16x32_bf16 v[128:131], a[224:227], v[52:55], v[128:131]\n"
      "v_add_f32 v214, v214, v84\n"
      "v_add_f32 v215, v215, v85\n"
      "v_mfma_f32_16x16x32_bf16 v[132:135], a[228:231], v[52:55], v[132:135]\n"
      "v_add_f32 v216, v216, v86\n"
      "v_add_f32 v217, v217, v87\n"
      "v_mfma_f32_16x16x32_bf16 v[136:139], a[232:235], v[52:55], v[136:139]\n"
      "v_add_f32 v218, v218, v88\n"
      "s_waitcnt vmcnt(10)\n"
      "v_mfma_f32_16x16x32_bf16 v[120:123], a[236:239], v[56:59], v[120:123]\n"
      "buffer_load_dwordx4 a[56:59], v254, s[12:15], s59 offen offset:2048 sc1\n"
      "v_add_f32 v219, v219, v89\n"
      "v_mfma_f32_16x16x32_bf16 v[124:127], a[240:243], v[56:59], v[124:127]\n"
      "v_add_f32 v220, v220, v90\n"
      "v_add_f32 v221, v221, v91\n"
      "v_mfma_f32_16x16x32_bf16 v[128:131], a[244:247], v[56:59], v[128:131]\n"
      "v_add_f32 v222, v222, v92\n"
      "v_add_f32 v223, v223, v93\n"
      "v_mfma_f32_16x16x32_bf16 v[132:135], a[248:251], v[56:59], v[132:135]\n"
      "v_add_f32 v224, v224, v94\n"
      "v_add_f32 v225, v225, v95\n"
      "v_mfma_f32_16x16x32_bf16 v[136:139], a[252:255], v[56:59], v[136:139]\n"
      "v_add_f32 v226, v226, v96\n"
      "v_add_f32 v227, v227, v97\n"
      "v_add_f32 v228, v228, v98\n"
      "v_add_f32 v229, v229, v99\n"
      "s_waitcnt vmcnt(10)\n"
      "v_mfma_f32_16x16x32_bf16 v[210:213], a[156:159], v[60:63], v[210:213]\n"
      "buffer_load_dwordx4 a[60:63], v254, s[12:15], s59 offen offset:3072 sc1\n"
      "v_mfma_f32_16x16x32_bf16 v[214:217], a[160:163], v[60:63], v[214:217]\n"
      "v_mfma_f32_16x16x32_bf16 v[218:221], a[164:167], v[60:63], v[218:221]\n"
      "ds_write_b128 v204, v[120:123] offset:0\n"
      "v_mfma_f32_16x16x32_bf16 v[222:225], a[168:171], v[60:63], v[222:225]\n"
      "v_mfma_f32_16x16x32_bf16 v[226:229], a[172:175], v[60:63], v[226:229]\n"
      "s_waitcnt vmcnt(10)\n"
      "v_mfma_f32_16x16x32_bf16 v[210:213], a[176:179], v[64:67], v[210:213]\n"
      "s_add_u32 s59, s59, 0x1000\n"
      "buffer_load_dwordx4 a[64:67], v254, s[12:15], s59 offen offset:0 sc1\n"
      "ds_write_b128 v204, v[124:127] offset:1024\n"
      "v_mfma_f32_16x16x32_bf16 v[214:217], a[180:183], v[64:67], v[214:217]\n"
      "v_mfma_f32_16x16x32_bf16 v[218:221], a[184:187], v[64:67], v[218:221]\n"
      "v_mfma_f32_16x16x32_bf16 v[222:225], a[188:191], v[64:67], v[222:225]\n"
      "ds_write_b128 v204, v[128:131] offset:2048\n"
      "v_mfma_f32_16x16x32_bf16 v[226:229], a[192:195], v[64:67], v[226:229]\n"
      "s_waitcnt vmcnt(10)\n"
      "v_mfma_f32_16x16x32_bf16 v[210:213], a[196:199], v[68:71], v[210:213]\n"
      "buffer_load_dwordx4 a[68:71], v254, s[12:15], s59 offen offset:1024 sc1\n"
      "v_mfma_f32_16x16x32_bf16 v[214:217], a[200:203], v[68:71], v[214:217]\n"
      "ds_write_b128 v204, v[132:135] offset:3072\n"
      "v_mfma_f32_16x16x32_bf16 v[218:221], a[204:207], v[68:71], v[218:221]\n"
      "v_mfma_f32_16x16x32_bf16 v[222:225], a[208:211], v[68:71], v[222:225]\n"
      "buffer_load_dwordx4 a[72:75], v254, s[12:15], s59 offen offset:2048 sc1\n"
      "v_mfma_f32_16x16x32_bf16 v[226:229], a[212:215], v[68:71], v[226:229]\n"
      "ds_write_b128 v204, v[136:139] offset:4096\n"
      "s_waitcnt vmcnt(10)\n"
      "v_mfma_f32_16x16x32_bf16 v[210:213], a[216:219], v[72:75], v[210:213]\n"
      "v_mfma_f32_16x16x32_bf16 v[214:217], a[220:223], v[72:75], v[214:217]\n"
      "v_mfma_f32_16x16x32_bf16 v[218:221], a[224:227], v[72:75], v[218:221]\n"
      "buffer_load_dwordx4 a[76:79], v254, s[12:15], s59 offen offset:3072 sc1\n"
      "v_mfma_f32_16x16x32_bf16 v[222:225], a[228:231], v[72:75], v[222:225]\n"
      "s_waitcnt lgkmcnt(0)\n"
      "s_barrier\n"
      "v_mfma_f32_16x16x32_bf16 v[226:229], a[232:235], v[72:75], v[226:229]\n"
      "ds_read_b128 v[100:103], v205 offset:5120\n"
      "s_waitcnt vmcnt(8)\n"
      "v_mfma_f32_16x16x32_bf16 v[210:213], a[236:239], v[76:79], v[210:213]\n"
      "ds_read_b128 v[120:123], v205 offset:10240\n"
      "ds_read_b128 v[104:107], v205 offset:6144\n"
      "v_mfma_f32_16x16x32_bf16 v[214:217], a[240:243], v[76:79], v[214:217]\n"
      "ds_read_b128 v[124:127], v205 offset:11264\n"
      "ds_read_b128 v[108:111], v205 offset:7168\n"
      "v_mfma_f32_16x16x32_bf16 v[218:221], a[244:247], v[76:79], v[218:221]\n"
      "ds_read_b128 v[128:131], v205 offset:12288\n"
      "ds_read_b128 v[112:115], v205 offset:8192\n"
      "v_mfma_f32_16x16x32_bf16 v[222:225], a[248:251], v[76:79], v[222:225]\n"
      "ds_read_b128 v[132:135], v205 offset:13312\n"
      "ds_read_b128 v[116:119], v205 offset:9216\n"
      "v_mfma_f32_16x16x32_bf16 v[226:229], a[252:255], v[76:79], v[226:229]\n"
      "ds_read_b128 v[136:139], v205 offset:14336\n"
      "s_waitcnt lgkmcnt(8)\n"
      "v_add_f32 v100, v100, v120\n"
      "v_add_f32 v101, v101, v121\n"
      "v_add_f32 v102, v102, v122\n"
      "v_add_f32 v103, v103, v123\n"
      "v_add_f32 v210, v210, v100\n"
      "v_add_f32 v211, v211, v101\n"
      "v_add_f32 v212, v212, v102\n"
      "v_add_f32 v213, v213, v103\n"
      "v_cvt_pk_bf16_f32 v182, v210, v211\n"
      "v_cvt_pk_bf16_f32 v183, v212, v213\n"
      "s_waitcnt lgkmcnt(6)\n"
      "v_add_f32 v104, v104, v124\n"
      "v_add_f32 v105, v105, v125\n"
      "v_add_f32 v106, v106, v126\n"
      "v_add_f32 v107, v107, v127\n"
      "v_add_f32 v214, v214, v104\n"
      "v_add_f32 v215, v215, v105\n"
      "v_add_f32 v216, v216, v106\n"
      "v_add_f32 v217, v217, v107\n"
      "v_cvt_pk_bf16_f32 v184, v214, v215\n"
      "v_cvt_pk_bf16_f32 v185, v216, v217\n"
      "buffer_store_dwordx4 v[182:185], v254, s[12:15], s92 offen sc1\n"
      "s_waitcnt lgkmcnt(4)\n"
      "v_add_f32 v108, v108, v128\n"
      "v_add_f32 v109, v109, v129\n"
      "v_add_f32 v110, v110, v130\n"
      "v_add_f32 v111, v111, v131\n"
      "v_add_f32 v218, v218, v108\n"
      "v_add_f32 v219, v219, v109\n"
      "v_add_f32 v220, v220, v110\n"
      "v_add_f32 v221, v221, v111\n"
      "v_cvt_pk_bf16_f32 v186, v218, v219\n"
      "v_cvt_pk_bf16_f32 v187, v220, v221\n"
      "s_waitcnt lgkmcnt(2)\n"
      "v_add_f32 v112, v112, v132\n"
      "v_add_f32 v113, v113, v133\n"
      "v_add_f32 v114, v114, v134\n"
      "v_add_f32 v115, v115, v135\n"
      "v_add_f32 v222, v222, v112\n"
      "v_add_f32 v223, v223, v113\n"
      "v_add_f32 v224, v224, v114\n"
      "v_add_f32 v225, v225, v115\n"
      "v_cvt_pk_bf16_f32 v188, v222, v223\n"
      "v_cvt_pk_bf16_f32 v189, v224, v225\n"
      "buffer_store_dwordx4 v[186:189], v254, s[12:15], s92 offen offset:1024 sc1\n"
      "s_waitcnt lgkmcnt(0)\n"
      "v_add_f32 v116, v116, v136\n"
      "v_add_f32 v117, v117, v137\n"
      "v_add_f32 v118, v118, v138\n"
      "v_add_f32 v119, v119, v139\n"
      "v_add_f32 v226, v226, v116\n"
      "v_add_f32 v227, v227, v117\n"
      "v_add_f32 v228, v228, v118\n"
      "v_add_f32 v229, v229, v119\n"
      "v_cvt_pk_bf16_f32 v190, v226, v227\n"
      "v_cvt_pk_bf16_f32 v191, v228, v229\n"
      "v_mov_b32 v0, v190\n"
      "v_mov_b32 v1, v191\n"
      "v_dot2_f32_bf16 v4, v182, v198, 0\n"
      "v_dot2_f32_bf16 v5, v182, v182, 0\n"
      "v_permlane16_swap_b32 v0, v192\n"
      "v_permlane16_swap_b32 v1, v193\n"
      "s_mov_b32 exec_lo, 0xffff\n"
      "s_mov_b32 exec_hi, 0xffff\n"
      "buffer_store_dwordx4 v[190:193], v196, s[12:15], s92 offen offset:2048 sc1\n"
      "s_mov_b64 exec, -1\n"
      "v_dot2_f32_bf16 v4, v183, v198, v4\n"
      "v_dot2_f32_bf16 v5, v183, v183, v5\n"
      "v_dot2_f32_bf16 v4, v184, v198, v4\n"
      "v_dot2_f32_bf16 v5, v184, v184, v5\n"
      "v_dot2_f32_bf16 v4, v185, v198, v4\n"
      "v_dot2_f32_bf16 v5, v185, v185, v5\n"
      "v_dot2_f32_bf16 v4, v186, v198, v4\n"
      "v_dot2_f32_bf16 v5, v186, v186, v5\n"
      "v_dot2_f32_bf16 v4, v187, v198, v4\n"
      "v_dot2_f32_bf16 v5, v187, v187, v5\n"
      "v_dot2_f32_bf16 v4, v188, v198, v4\n"
      "v_dot2_f32_bf16 v5, v188, v188, v5\n"
      "v_dot2_f32_bf16 v4, v189, v198, v4\n"
      "v_dot2_f32_bf16 v5, v189, v189, v5\n"
      "v_dot2_f32_bf16 v4, v190, v198, v4\n"
      "v_dot2_f32_bf16 v5, v190, v190, v5\n"
      "v_dot2_f32_bf16 v4, v191, v198, v4\n"
      "v_dot2_f32_bf16 v5, v191, v191, v5\n"
      "v_accvgpr_write_b32 a244, v182\n"
      "v_accvgpr_write_b32 a245, v183\n"
      "v_accvgpr_write_b32 a246, v184\n"
      "v_permlane32_swap_b32 v4, v5\n"
      "v_add_f32 v4, v4, v5\n"
      "v_mov_b32 v5, v4\n"
      "v_accvgpr_write_b32 a247, v185\n"
      "v_accvgpr_write_b32 a248, v186\n"
      "v_permlane16_swap_b32 v4, v5\n"
      "v_add_f32 v4, v4, v5\n"
      "s_mov_b32 exec_lo, 0xffff\n"
      "s_mov_b32 exec_hi, 0xffff\n"
      "buffer_store_dword v4, v197, s[12:15], s93 offen sc1\n"
      "s_mov_b64 exec, -1\n"
      "v_accvgpr_write_b32 a249, v187\n"
      "v_accvgpr_write_b32 a250, v188\n"
      "v_accvgpr_write_b32 a251, v189\n"
      "v_accvgpr_write_b32 a252, v190\n"
      "v_accvgpr_write_b32 a253, v191\n"
      "s_lshl4_add_u32 s44, s3, s35\n"
      "s_lshl_b32 s44, s44, 1\n"
      "s_min_u32 s46, s35, 11\n"
      "s_add_u32 s46, s46, 96\n"
      "s_cmp_eq_u32 s3, 3\n"
      "s_cselect_b32 s44, s46, s44\n"
      "s_cselect_b32 exec_lo, 0xffff, -1\n"
      "s_lshl_b32 s44, s44, 9\n"
      "s_add_u32 s44, s44, 0x664000\n"
      "s_mov_b32 exec_hi, 0\n"
      "v_lshlrev_b32 v9, 1, v254\n"
      "buffer_load_dwordx4 v[12:15], v9, s[12:15], s44 offen sc1\n"
      "s_mov_b64 exec, -1\n"
      "s_sub_u32 s59, s59, s84\n"
      "s_add_u32 s59, s59, 0x38000\n"
      "buffer_load_dwordx4 a[80:83], v251, s[12:15], s59 offen offset:0 sc1\n"
      "buffer_load_dwordx4 a[84:87], v251, s[12:15], s59 offen offset:1024 sc1\n"
      "buffer_load_dwordx4 a[88:91], v251, s[12:15], s59 offen offset:2048 sc1\n"
      "buffer_load_dwordx4 a[92:95], v251, s[12:15], s59 offen offset:3072 sc1\n"
      "v_mov_b32 v246, v254\n"
      "v_add_u32 v247, 61440, v254\n"
      "v_add_u32 v248, 122880, v254\n"
      "v_and_b32 v8, 1, v253\n"
      "v_lshrrev_b32 v9, 1, v253\n"
      "v_mul_u32_u24 v8, 55296, v8\n"
      "v_lshl_add_u32 v8, v9, 8, v8\n"
      "v_lshl_add_u32 v249, v252, 4, v8\n"
      "s_lshl2_add_u32 s44, s34, s3\n"
      "s_mul_i32 s60, s44, 40960\n"
      "s_add_u32 s60, s60, 0xb10000\n"
      "s_add_u32 s61, s60, 20480\n"
      "s_lshl_b32 s62, s44, 11\n"
      "s_add_u32 s62, s62, 0xd90000\n"
      "s_mul_i32 s44, s44, 110592\n"
      "s_add_u32 s44, s44, 0xdb0000\n"
      "s_lshl_b32 s45, s35, 10\n"
      "s_add_u32 s70, s44, s45\n"
      "s_add_u32 s71, s70, 16384\n"
      "s_add_u32 s72, s70, 32768\n"
      "s_min_u32 s45, s35, 11\n"
      "s_lshl_b32 s45, s45, 9\n"
      "s_add_u32 s44, s44, s45\n"
      "s_add_u32 s73, s44, 49152\n"
      "s_cmp_lt_u32 s35, 12\n"
      "s_cselect_b32 s63, 0, 0x40000000\n"
      "v_lshrrev_b32 v230, 1, v254\n"
      "s_mov_b32 s95, 0xbfb8aa3b\n"
      "s_mul_i32 s45, s3, 4096\n"
      "s_add_u32 s45, s45, 0xdb0000\n"
      "s_add_u32 s44, s3, 1\n"
      "s_and_b32 s44, s44, 3\n"
      "s_lshl2_add_u32 s44, s37, s44\n"
      "s_mul_i32 s44, s44, 55296\n"
      "s_add_u32 s64, s44, s45\n"
      "s_add_u32 s44, s3, 2\n"
      "s_and_b32 s44, s44, 3\n"
      "s_lshl2_add_u32 s44, s37, s44\n"
      "s_mul_i32 s44, s44, 55296\n"
      "s_add_u32 s65, s44, s45\n"
      "s_add_u32 s44, s3, 3\n"
      "s_and_b32 s44, s44, 3\n"
      "s_lshl2_add_u32 s44, s37, s44\n"
      "s_mul_i32 s44, s44, 55296\n"
      "s_add_u32 s66, s44, s45\n"
      "s_add_u32 s44, s3, 4\n"
      "s_and_b32 s44, s44, 3\n"
      "s_lshl2_add_u32 s44, s37, s44\n"
      "s_mul_i32 s44, s44, 55296\n"
      "s_add_u32 s67, s44, s45\n"
      "s_lshr_b32 s44, s3, 1\n"
      "s_lshl1_add_u32 s44, s34, s44\n"
      "s_lshl_b32 s44, s44, 8\n"
      "s_add_u32 s45, s44, 0xa0000\n"
      "v_lshlrev_b32 v2, 1, v254\n"
      "s_lshl_b32 s43, s85, 5\n"
      "s_add_u32 s43, s43, 0xa0000\n"
      "v_mov_b32 v30, s40\n"
      "v_mov_b32 v31, s41\n"
      "v_mov_b32 v32, s40\n"
      "v_mov_b32 v33, s41\n"
      "v_add_u32 v34, s42, v254\n"
      "s_mov_b64 exec, 0x3\n"
      "s_waitcnt vmcnt(5)\n"
      "s_barrier\n"
      "buffer_store_dwordx4 v[30:33], v34, s[12:15], s43 offen sc1\n"
      "s_mov_b64 exec, -1\n"
      "s_add_u32 s81, s58, 1310720\n"
      "s_add_u32 m0, s79, 102400\n"
      "s_nop 0\n"
      "buffer_load_dwordx4 v254, s[12:15], s81 offen offset:0 sc1 lds\n"
      "buffer_load_dwordx4 v254, s[12:15], s81 offen offset:1024 sc1 lds\n"
      "buffer_load_dwordx4 v254, s[12:15], s81 offen offset:2048 sc1 lds\n"
      "buffer_load_dwordx4 v254, s[12:15], s81 offen offset:3072 sc1 lds\n"
      "s_add_u32 s81, s58, 1314816\n"
      "s_add_u32 m0, s79, 106496\n"
      "s_nop 0\n"
      "buffer_load_dwordx4 v254, s[12:15], s81 offen offset:0 sc1 lds\n"
      "s_add_u32 s81, s58, 1331200\n"
      "s_add_u32 m0, s79, 122880\n"
      "s_nop 0\n"
      "buffer_load_dwordx4 v254, s[12:15], s81 offen offset:0 sc1 lds\n"
      "buffer_load_dwordx4 v254, s[12:15], s81 offen offset:1024 sc1 lds\n"
      "buffer_load_dwordx4 v254, s[12:15], s81 offen offset:2048 sc1 lds\n"
      "buffer_load_dwordx4 v254, s[12:15], s81 offen offset:3072 sc1 lds\n"
      "s_add_u32 s81, s58, 1335296\n"
      "s_add_u32 m0, s79, 126976\n"
      "s_nop 0\n"
      "buffer_load_dwordx4 v254, s[12:15], s81 offen offset:0 sc1 lds\n"
      "s_add_u32 s59, s59, 0x1000\n"
      "buffer_load_dwordx4 a[96:99], v251, s[12:15], s59 offen offset:0 sc1\n"
      "buffer_load_dwordx4 a[100:103], v251, s[12:15], s59 offen offset:1024 sc1\n"
      "buffer_load_dwordx4 a[104:107], v251, s[12:15], s59 offen offset:2048 sc1\n"
      "s_nop 0\n"
      ".Lk_pad4:\n"
      "s_cbranch_execz .Lk_hopret\n"
      "buffer_load_dwordx4 a[108:111], v251, s[12:15], s59 offen offset:3072 sc1\n"
      "s_waitcnt vmcnt(19)\n"
      "v_add_f32_dpp v12, v12, v12 row_ror:4 row_mask:0xf bank_mask:0xf\n"
      "v_add_f32_dpp v13, v13, v13 row_ror:4 row_mask:0xf bank_mask:0xf\n"
      "v_add_f32_dpp v14, v14, v14 row_ror:4 row_mask:0xf bank_mask:0xf\n"
      "v_add_f32_dpp v15, v15, v15 row_ror:4 row_mask:0xf bank_mask:0xf\n"
      "v_add_f32_dpp v12, v12, v12 row_ror:8 row_mask:0xf bank_mask:0xf\n"
      "v_add_f32_dpp v13, v13, v13 row_ror:8 row_mask:0xf bank_mask:0xf\n"
      "v_add_f32_dpp v14, v14, v14 row_ror:8 row_mask:0xf bank_mask:0xf\n"
      "v_add_f32_dpp v15, v15, v15 row_ror:8 row_mask:0xf bank_mask:0xf\n"
      "s_lshl_b32 s44, s3, 7\n"
      "s_add_u32 s44, s44, 143360\n"
      "v_lshl_add_u32 v8, v253, 6, s44\n"
      "v_lshl_add_u32 v8, v252, 4, v8\n"
      "s_mov_b64 exec, 0xf000f\n"
      "ds_write_b128 v8, v[12:15]\n"
      "s_mov_b64 exec, -1\n"
      "v_lshlrev_b32 v250, 4, v253\n"
      "v_add_u32 v250, 143360, v250\n"
      "s_mov_b64 exec, 0xff\n"
      "buffer_load_dwordx2 v[0:1], v2, s[12:15], s45 offen sc1\n"
      "s_mov_b64 exec, -1\n"
      "s_add_u32 s59, s59, 0x1000\n"
      "buffer_load_dwordx4 a[112:115], v251, s[12:15], s59 offen offset:0 sc1\n"
      "buffer_load_dwordx4 a[116:119], v251, s[12:15], s59 offen offset:1024 sc1\n"
      "s_add_u32 s59, s59, s84\n"
      "s_add_u32 s59, s59, 0xfffd6000\n"
      "buffer_load_dwordx4 a[120:123], v254, s[12:15], s59 offen offset:0 sc1\n"
      "buffer_load_dwordx4 a[124:127], v254, s[12:15], s59 offen offset:1024 sc1\n"
      "s_waitcnt vmcnt(4)\n"
      "v_cmp_ne_u64 vcc, s[40:41], v[0:1]\n"
      "s_and_b32 vcc_lo, vcc_lo, 0xff\n"
      "s_cbranch_scc0 .Lk_ok_9\n"
      "s_mov_b64 exec, 0xff\n"
      ".Lk_poll_10:\n"
      "s_sleep 1\n"
      "buffer_load_dwordx2 v[0:1], v2, s[12:15], s45 offen sc1\n"
      "s_waitcnt vmcnt(0)\n"
      "v_cmp_ne_u64 vcc, s[40:41], v[0:1]\n"
      "s_cbranch_vccz .Lk_polld_11\n"
      "s_cmp_eq_u32 s39, 0\n"
      "s_cbranch_scc1 .Lk_polld_11\n"
      "s_sub_u32 s39, s39, 1\n"
      "s_branch .Lk_poll_10\n"
      ".Lk_polld_11:\n"
      "s_mov_b64 exec, -1\n"
      ".Lk_ok_9:\n"
      "buffer_load_dwordx4 v[0:3], v254, s[12:15], s60 offen offset:0 sc1\n"
      "buffer_load_dwordx4 v[80:83], v254, s[12:15], s61 offen offset:0 sc1\n"
      "buffer_load_dwordx2 v[232:233], v230, s[12:15], s62 offen offset:0 sc1\n"
      "buffer_load_dwordx2 v[234:235], v230, s[12:15], s62 offen offset:512 sc1\n"
      "buffer_load_dwordx2 v[236:237], v230, s[12:15], s62 offen offset:1024 sc1\n"
      "buffer_load_dwordx2 v[238:239], v230, s[12:15], s62 offen offset:1536 sc1\n"
      "s_waitcnt lgkmcnt(0)\n"
      "s_barrier\n"
      "ds_read_b128 a[212:215], v246 offset:0\n"
      "ds_read_b128 a[216:219], v246 offset:20480\n"
      "ds_read_b128 a[220:223], v246 offset:40960\n"
      "ds_read_b128 a[224:227], v247 offset:0\n"
      "buffer_load_dwordx4 v[4:7], v254, s[12:15], s60 offen offset:1024 sc1\n"
      "buffer_load_dwordx4 v[84:87], v254, s[12:15], s61 offen offset:1024 sc1\n"
      "buffer_load_dwordx4 v[8:11], v254, s[12:15], s60 offen offset:2048 sc1\n"
      "buffer_load_dwordx4 v[88:91], v254, s[12:15], s61 offen offset:2048 sc1\n"
      "buffer_load_dwordx4 v[12:15], v254, s[12:15], s60 offen offset:3072 sc1\n"
      "buffer_load_dwordx4 v[92:95], v254, s[12:15], s61 offen offset:3072 sc1\n"
      "s_add_u32 s60, s60, 0x1000\n"
      "buffer_load_dwordx4 v[16:19], v254, s[12:15], s60 offen offset:0 sc1\n"
      "s_add_u32 s61, s61, 0x1000\n"
      "buffer_load_dwordx4 v[96:99], v254, s[12:15], s61 offen offset:0 sc1\n"
      "buffer_load_dwordx4 v[20:23], v254, s[12:15], s60 offen offset:1024 sc1\n"
      "buffer_load_dwordx4 v[100:103], v254, s[12:15], s61 offen offset:1024 sc1\n"
      "buffer_load_dwordx4 v[24:27], v254, s[12:15], s60 offen offset:2048 sc1\n"
      "buffer_load_dwordx4 v[104:107], v254, s[12:15], s61 offen offset:2048 sc1\n"
      "s_waitcnt vmcnt(16) lgkmcnt(0)\n"
      "v_mfma_f32_16x16x32_bf16 v[160:163], a[212:215], v[0:3], 0\n"
      "ds_read_b128 a[228:231], v246 offset:1024\n"
      "v_mfma_f32_16x16x32_bf16 v[164:167], a[212:215], v[80:83], 0\n"
      "ds_read_b128 a[232:235], v246 offset:21504\n"
      "v_mfma_f32_16x16x32_bf16 v[172:175], a[216:219], v[80:83], 0\n"
      "ds_read_b128 a[236:239], v246 offset:41984\n"
      "v_mfma_f32_16x16x32_bf16 v[168:171], a[216:219], v[0:3], 0\n"
      "ds_read_b128 a[240:243], v247 offset:1024\n"
      "v_mfma_f32_16x16x32_bf16 v[176:179], a[220:223], v[0:3], 0\n"
      "v_mfma_f32_16x16x32_bf16 v[180:183], a[220:223], v[80:83], 0\n"
      "buffer_load_dwordx4 v[28:31], v254, s[12:15], s60 offen offset:3072 sc1\n"
      "v_mfma_f32_16x16x32_bf16 v[188:191], a[224:227], v[80:83], 0\n"
      "v_mfma_f32_16x16x32_bf16 v[184:187], a[224:227], v[0:3], 0\n"
      "buffer_load_dwordx4 v[108:111], v254, s[12:15], s61 offen offset:3072 sc1\n"
      "s_waitcnt vmcnt(12) lgkmcnt(0)\n"
      "v_mfma_f32_16x16x32_bf16 v[160:163], a[228:231], v[4:7], v[160:163]\n"
      "ds_read_b128 a[212:215], v246 offset:2048\n"
      "ds_read_b128 v[222:225], v250\n"
      "v_mfma_f32_16x16x32_bf16 v[164:167], a[228:231], v[84:87], v[164:167]\n"
      "ds_read_b128 a[216:219], v246 offset:22528\n"
      "ds_read_b128 v[226:229], v250 offset:64\n"
      "v_mfma_f32_16x16x32_bf16 v[172:175], a[232:235], v[84:87], v[172:175]\n"
      "ds_read_b128 a[220:223], v246 offset:43008\n"
      "v_add_f32 v232, v232, v234\n"
      "v_mfma_f32_16x16x32_bf16 v[168:171], a[232:235], v[4:7], v[168:171]\n"
      "ds_read_b128 a[224:227], v247 offset:2048\n"
      "v_add_f32 v233, v233, v235\n"
      "v_mfma_f32_16x16x32_bf16 v[176:179], a[236:239], v[4:7], v[176:179]\n"
      "v_add_f32 v236, v236, v238\n"
      "v_add_f32 v237, v237, v239\n"
      "v_mfma_f32_16x16x32_bf16 v[180:183], a[236:239], v[84:87], v[180:183]\n"
      "s_add_u32 s60, s60, 0x1000\n"
      "buffer_load_dwordx4 v[32:35], v254, s[12:15], s60 offen offset:0 sc1\n"
      "v_mfma_f32_16x16x32_bf16 v[188:191], a[240:243], v[84:87], v[188:191]\n"
      "v_mov_b32 v234, v232\n"
      "v_mov_b32 v235, v233\n"
      "v_mfma_f32_16x16x32_bf16 v[184:187], a[240:243], v[4:7], v[184:187]\n"
      "s_add_u32 s61, s61, 0x1000\n"
      "buffer_load_dwordx4 v[112:115], v254, s[12:15], s61 offen offset:0 sc1\n"
      "s_waitcnt vmcnt(12) lgkmcnt(0)\n"
      "v_mfma_f32_16x16x32_bf16 v[160:163], a[212:215], v[8:11], v[160:163]\n"
      "ds_read_b128 a[228:231], v246 offset:3072\n"
      "v_mov_b32 v238, v236\n"
      "v_mfma_f32_16x16x32_bf16 v[164:167], a[212:215], v[88:91], v[164:167]\n"
      "ds_read_b128 a[232:235], v246 offset:23552\n"
      "v_mov_b32 v239, v237\n"
      "v_mfma_f32_16x16x32_bf16 v[172:175], a[216:219], v[88:91], v[172:175]\n"
      "ds_read_b128 a[236:239], v246 offset:44032\n"
      "v_permlane32_swap_b32 v232, v234\n"
      "v_mfma_f32_16x16x32_bf16 v[168:171], a[216:219], v[8:11], v[168:171]\n"
      "ds_read_b128 a[240:243], v247 offset:3072\n"
      "v_permlane32_swap_b32 v233, v235\n"
      "v_mfma_f32_16x16x32_bf16 v[176:179], a[220:223], v[8:11], v[176:179]\n"
      "v_permlane32_swap_b32 v236, v238\n"
      "v_permlane32_swap_b32 v237, v239\n"
      "v_mfma_f32_16x16x32_bf16 v[180:183], a[220:223], v[88:91], v[180:183]\n"
      "buffer_load_dwordx4 v[36:39], v254, s[12:15], s60 offen offset:1024 sc1\n"
      "v_add_f32 v232, v232, v234\n"
      "v_mfma_f32_16x16x32_bf16 v[188:191], a[224:227], v[88:91], v[188:191]\n"
      "v_add_f32 v233, v233, v235\n"
      "v_add_f32 v236, v236, v238\n"
      "v_mfma_f32_16x16x32_bf16 v[184:187], a[224:227], v[8:11], v[184:187]\n"
      "buffer_load_dwordx4 v[116:119], v254, s[12:15], s61 offen offset:1024 sc1\n"
      "v_add_f32 v237, v237, v239\n"
      "s_waitcnt vmcnt(12) lgkmcnt(0)\n"
      "v_mfma_f32_16x16x32_bf16 v[160:163], a[228:231], v[12:15], v[160:163]\n"
      "ds_read_b128 a[212:215], v246 offset:4096\n"
      "v_mov_b32 v234, v232\n"
      "v_mfma_f32_16x16x32_bf16 v[164:167], a[228:231], v[92:95], v[164:167]\n"
      "ds_read_b128 a[216:219], v246 offset:24576\n"
      "v_mov_b32 v235, v233\n"
      "v_mfma_f32_16x16x32_bf16 v[172:175], a[232:235], v[92:95], v[172:175]\n"
      "ds_read_b128 a[220:223], v246 offset:45056\n"
      "v_mov_b32 v238, v236\n"
      "v_mfma_f32_16x16x32_bf16 v[168:171], a[232:235], v[12:15], v[168:171]\n"
      "ds_read_b128 a[224:227], v247 offset:4096\n"
      "v_mov_b32 v239, v237\n"
      "v_mfma_f32_16x16x32_bf16 v[176:179], a[236:239], v[12:15], v[176:179]\n"
      "v_permlane16_swap_b32 v232, v234\n"
      "v_permlane16_swap_b32 v233, v235\n"
      "v_mfma_f32_16x16x32_bf16 v[180:183], a[236:239], v[92:95], v[180:183]\n"
      "buffer_load_dwordx4 v[40:43], v254, s[12:15], s60 offen offset:2048 sc1\n"
      "v_permlane16_swap_b32 v236, v238\n"
      "v_mfma_f32_16x16x32_bf16 v[188:191], a[240:243], v[92:95], v[188:191]\n"
      "v_permlane16_swap_b32 v237, v239\n"
      "v_add_f32 v232, v232, v234\n"
      "v_mfma_f32_16x16x32_bf16 v[184:187], a[240:243], v[12:15], v[184:187]\n"
      "buffer_load_dwordx4 v[120:123], v254, s[12:15], s61 offen offset:2048 sc1\n"
      "v_add_f32 v233, v233, v235\n"
      "s_waitcnt vmcnt(12) lgkmcnt(0)\n"
      "v_mfma_f32_16x16x32_bf16 v[160:163], a[212:215], v[16:19], v[160:163]\n"
      "ds_read_b128 a[228:231], v246 offset:5120\n"
      "v_add_f32 v236, v236, v238\n"
      "v_mfma_f32_16x16x32_bf16 v[164:167], a[212:215], v[96:99], v[164:167]\n"
      "ds_read_b128 a[232:235], v246 offset:25600\n"
      "v_add_f32 v237, v237, v239\n"
      "v_mfma_f32_16x16x32_bf16 v[172:175], a[216:219], v[96:99], v[172:175]\n"
      "ds_read_b128 a[236:239], v246 offset:46080\n"
      "v_mul_f32 v216, 0xbacccccd, v232\n"
      "v_mfma_f32_16x16x32_bf16 v[168:171], a[216:219], v[16:19], v[168:171]\n"
      "ds_read_b128 a[240:243], v247 offset:5120\n"
      "v_mul_f32 v233, 0x3acccccd, v233\n"
      "v_mfma_f32_16x16x32_bf16 v[176:179], a[220:223], v[16:19], v[176:179]\n"
      "v_fma_f32 v233, -v216, v216, v233\n"
      "v_max_f32 v233, 0, v233\n"
      "v_mfma_f32_16x16x32_bf16 v[180:183], a[220:223], v[96:99], v[180:183]\n"
      "buffer_load_dwordx4 v[44:47], v254, s[12:15], s60 offen offset:3072 sc1\n"
      "v_add_f32 v233, 0x3727c5ac, v233\n"
      "v_mfma_f32_16x16x32_bf16 v[188:191], a[224:227], v[96:99], v[188:191]\n"
      "v_rsq_f32 v232, v233\n"
      "v_mfma_f32_16x16x32_bf16 v[184:187], a[224:227], v[16:19], v[184:187]\n"
      "buffer_load_dwordx4 v[124:127], v254, s[12:15], s61 offen offset:3072 sc1\n"
      "v_mul_f32 v218, s95, v232\n"
      "s_waitcnt vmcnt(12) lgkmcnt(0)\n"
      "v_mfma_f32_16x16x32_bf16 v[160:163], a[228:231], v[20:23], v[160:163]\n"
      "ds_read_b128 a[212:215], v246 offset:6144\n"
      "v_mul_f32 v217, 0xbacccccd, v236\n"
      "v_mfma_f32_16x16x32_bf16 v[164:167], a[228:231], v[100:103], v[164:167]\n"
      "ds_read_b128 a[216:219], v246 offset:26624\n"
      "v_mul_f32 v237, 0x3acccccd, v237\n"
      "v_mfma_f32_16x16x32_bf16 v[172:175], a[232:235], v[100:103], v[172:175]\n"
      "ds_read_b128 a[220:223], v246 offset:47104\n"
      "v_fma_f32 v237, -v217, v217, v237\n"
      "v_mfma_f32_16x16x32_bf16 v[168:171], a[232:235], v[20:23], v[168:171]\n"
      "ds_read_b128 a[224:227], v247 offset:6144\n"
      "v_max_f32 v237, 0, v237\n"
      "v_mfma_f32_16x16x32_bf16 v[176:179], a[236:239], v[20:23], v[176:179]\n"
      "v_add_f32 v237, 0x3727c5ac, v237\n"
      "v_mfma_f32_16x16x32_bf16 v[180:183], a[236:239], v[100:103], v[180:183]\n"
      "s_add_u32 s60, s60, 0x1000\n"
      "buffer_load_dwordx4 v[48:51], v254, s[12:15], s60 offen offset:0 sc1\n"
      "v_mfma_f32_16x16x32_bf16 v[188:191], a[240:243], v[100:103], v[188:191]\n"
      "v_rsq_f32 v236, v237\n"
      "v_mfma_f32_16x16x32_bf16 v[184:187], a[240:243], v[20:23], v[184:187]\n"
      "s_add_u32 s61, s61, 0x1000\n"
      "buffer_load_dwordx4 v[128:131], v254, s[12:15], s61 offen offset:0 sc1\n"
      "s_waitcnt vmcnt(12) lgkmcnt(0)\n"
      "v_mfma_f32_16x16x32_bf16 v[160:163], a[212:215], v[24:27], v[160:163]\n"
      "ds_read_b128 a[228:231], v246 offset:7168\n"
      "v_mul_f32 v219, s95, v236\n"
      "v_mfma_f32_16x16x32_bf16 v[164:167], a[212:215], v[104:107], v[164:167]\n"
      "ds_read_b128 a[232:235], v246 offset:27648\n"
      "v_mfma_f32_16x16x32_bf16 v[172:175], a[216:219], v[104:107], v[172:175]\n"
      "ds_read_b128 a[236:239], v246 offset:48128\n"
      "v_mfma_f32_16x16x32_bf16 v[168:171], a[216:219], v[24:27], v[168:171]\n"
      "ds_read_b128 a[240:243], v247 offset:7168\n"
      "v_fmac_f32 v160, v222, v216\n"
      "v_mfma_f32_16x16x32_bf16 v[176:179], a[220:223], v[24:27], v[176:179]\n"
      "v_fmac_f32 v161, v223, v216\n"
      "v_fmac_f32 v162, v224, v216\n"
      "v_mfma_f32_16x16x32_bf16 v[180:183], a[220:223], v[104:107], v[180:183]\n"
      "buffer_load_dwordx4 v[52:55], v254, s[12:15], s60 offen offset:1024 sc1\n"
      "v_fmac_f32 v163, v225, v216\n"
      "v_mfma_f32_16x16x32_bf16 v[188:191], a[224:227], v[104:107], v[188:191]\n"
      "v_fmac_f32 v164, v222, v217\n"
      "v_fmac_f32 v165, v223, v217\n"
      "v_mfma_f32_16x16x32_bf16 v[184:187], a[224:227], v[24:27], v[184:187]\n"
      "buffer_load_dwordx4 v[132:135], v254, s[12:15], s61 offen offset:1024 sc1\n"
      "v_fmac_f32 v166, v224, v217\n"
      "s_waitcnt vmcnt(12) lgkmcnt(0)\n"
      "v_mfma_f32_16x16x32_bf16 v[160:163], a[228:231], v[28:31], v[160:163]\n"
      "ds_read_b128 a[212:215], v246 offset:8192\n"
      "v_fmac_f32 v167, v225, v217\n"
      "s_nop 1\n"
      "v_mfma_f32_16x16x32_bf16 v[164:167], a[228:231], v[108:111], v[164:167]\n"
      "ds_read_b128 a[216:219], v246 offset:28672\n"
      "ds_read_b128 v[222:225], v250 offset:384\n"
      "v_mfma_f32_16x16x32_bf16 v[172:175], a[232:235], v[108:111], v[172:175]\n"
      "ds_read_b128 a[220:223], v246 offset:49152\n"
      "v_fmac_f32 v168, v226, v216\n"
      "s_nop 1\n"
      "v_mfma_f32_16x16x32_bf16 v[168:171], a[232:235], v[28:31], v[168:171]\n"
      "ds_read_b128 a[224:227], v247 offset:8192\n"
      "v_mfma_f32_16x16x32_bf16 v[176:179], a[236:239], v[28:31], v[176:179]\n"
      "v_mfma_f32_16x16x32_bf16 v[180:183], a[236:239], v[108:111], v[180:183]\n"
      "buffer_load_dwordx4 v[56:59], v254, s[12:15], s60 offen offset:2048 sc1\n"
      "v_mfma_f32_16x16x32_bf16 v[188:191], a[240:243], v[108:111], v[188:191]\n"
      "v_mfma_f32_16x16x32_bf16 v[184:187], a[240:243], v[28:31], v[184:187]\n"
      "buffer_load_dwordx4 v[136:139], v254, s[12:15], s61 offen offset:2048 sc1\n"
      "s_waitcnt vmcnt(12) lgkmcnt(0)\n"
      "v_mfma_f32_16x16x32_bf16 v[160:163], a[212:215], v[32:35], v[160:163]\n"
      "ds_read_b128 a[228:231], v246 offset:9216\n"
      "v_fmac_f32 v169, v227, v216\n"
      "v_mfma_f32_16x16x32_bf16 v[164:167], a[212:215], v[112:115], v[164:167]\n"
      "ds_read_b128 a[232:235], v246 offset:29696\n"
      "v_fmac_f32 v170, v228, v216\n"
      "v_mfma_f32_16x16x32_bf16 v[172:175], a[216:219], v[112:115], v[172:175]\n"
      "ds_read_b128 a[236:239], v246 offset:50176\n"
      "v_fmac_f32 v171, v229, v216\n"
      "s_nop 1\n"
      "v_mfma_f32_16x16x32_bf16 v[168:171], a[216:219], v[32:35], v[168:171]\n"
      "ds_read_b128 a[240:243], v247 offset:9216\n"
      "v_mfma_f32_16x16x32_bf16 v[176:179], a[220:223], v[32:35], v[176:179]\n"
      "v_mfma_f32_16x16x32_bf16 v[180:183], a[220:223], v[112:115], v[180:183]\n"
      "buffer_load_dwordx4 v[60:63], v254, s[12:15], s60 offen offset:3072 sc1\n"
      "v_fmac_f32 v172, v226, v217\n"
      "v_mfma_f32_16x16x32_bf16 v[188:191], a[224:227], v[112:115], v[188:191]\n"
      "v_fmac_f32 v173, v227, v217\n"
      "v_fmac_f32 v174, v228, v217\n"
      "v_mfma_f32_16x16x32_bf16 v[184:187], a[224:227], v[32:35], v[184:187]\n"
      "buffer_load_dwordx4 v[140:143], v254, s[12:15], s61 offen offset:3072 sc1\n"
      "v_fmac_f32 v175, v229, v217\n"
      "s_waitcnt vmcnt(12) lgkmcnt(0)\n"
      "v_mfma_f32_16x16x32_bf16 v[160:163], a[228:231], v[36:39], v[160:163]\n"
      "ds_read_b128 a[212:215], v246 offset:10240\n"
      "ds_read_b128 v[226:229], v250 offset:128\n"
      "v_mfma_f32_16x16x32_bf16 v[164:167], a[228:231], v[116:119], v[164:167]\n"
      "ds_read_b128 a[216:219], v246 offset:30720\n"
      "v_fmac_f32 v176, v222, v216\n"
      "v_mfma_f32_16x16x32_bf16 v[172:175], a[232:235], v[116:119], v[172:175]\n"
      "ds_read_b128 a[220:223], v246 offset:51200\n"
      "v_fmac_f32 v177, v223, v216\n"
      "v_mfma_f32_16x16x32_bf16 v[168:171], a[232:235], v[36:39], v[168:171]\n"
      "ds_read_b128 a[224:227], v247 offset:10240\n"
      "v_fmac_f32 v178, v224, v216\n"
      "s_nop 1\n"
      "v_mfma_f32_16x16x32_bf16 v[176:179], a[236:239], v[36:39], v[176:179]\n"
      "v_mfma_f32_16x16x32_bf16 v[180:183], a[236:239], v[116:119], v[180:183]\n"
      "s_add_u32 s60, s60, 0x1000\n"
      "buffer_load_dwordx4 v[64:67], v254, s[12:15], s60 offen offset:0 sc1\n"
      "v_mfma_f32_16x16x32_bf16 v[188:191], a[240:243], v[116:119], v[188:191]\n"
      "v_mfma_f32_16x16x32_bf16 v[184:187], a[240:243], v[36:39], v[184:187]\n"
      "s_add_u32 s61, s61, 0x1000\n"
      "buffer_load_dwordx4 v[144:147], v254, s[12:15], s61 offen offset:0 sc1\n"
      "s_waitcnt vmcnt(12) lgkmcnt(0)\n"
      "v_mfma_f32_16x16x32_bf16 v[160:163], a[212:215], v[40:43], v[160:163]\n"
      "ds_read_b128 a[228:231], v246 offset:11264\n"
      "v_fmac_f32 v179, v225, v216\n"
      "v_mfma_f32_16x16x32_bf16 v[164:167], a[212:215], v[120:123], v[164:167]\n"
      "ds_read_b128 a[232:235], v246 offset:31744\n"
      "v_fmac_f32 v180, v222, v217\n"
      "v_mfma_f32_16x16x32_bf16 v[172:175], a[216:219], v[120:123], v[172:175]\n"
      "ds_read_b128 a[236:239], v246 offset:52224\n"
      "v_fmac_f32 v181, v223, v217\n"
      "v_mfma_f32_16x16x32_bf16 v[168:171], a[216:219], v[40:43], v[168:171]\n"
      "ds_read_b128 a[240:243], v247 offset:11264\n"
      "v_fmac_f32 v182, v224, v217\n"
      "v_mfma_f32_16x16x32_bf16 v[176:179], a[220:223], v[40:43], v[176:179]\n"
      "v_fmac_f32 v183, v225, v217\n"
      "ds_read_b128 v[222:225], v250 offset:192\n"
      "s_nop 0\n"
      "v_mfma_f32_16x16x32_bf16 v[180:183], a[220:223], v[120:123], v[180:183]\n"
      "buffer_load_dwordx4 v[68:71], v254, s[12:15], s60 offen offset:1024 sc1\n"
      "v_fmac_f32 v184, v226, v216\n"
      "v_mfma_f32_16x16x32_bf16 v[188:191], a[224:227], v[120:123], v[188:191]\n"
      "v_fmac_f32 v185, v227, v216\n"
      "v_fmac_f32 v186, v228, v216\n"
      "s_nop 1\n"
      "v_mfma_f32_16x16x32_bf16 v[184:187], a[224:227], v[40:43], v[184:187]\n"
      "buffer_load_dwordx4 v[148:151], v254, s[12:15], s61 offen offset:1024 sc1\n"
      "s_waitcnt vmcnt(12) lgkmcnt(1)\n"
      "v_mfma_f32_16x16x32_bf16 v[160:163], a[228:231], v[44:47], v[160:163]\n"
      "ds_read_b128 a[212:215], v246 offset:12288\n"
      "v_mfma_f32_16x16x32_bf16 v[164:167], a[228:231], v[124:127], v[164:167]\n"
      "ds_read_b128 a[216:219], v246 offset:32768\n"
      "v_mfma_f32_16x16x32_bf16 v[172:175], a[232:235], v[124:127], v[172:175]\n"
      "ds_read_b128 a[220:223], v246 offset:53248\n"
      "v_fmac_f32 v187, v229, v216\n"
      "v_mfma_f32_16x16x32_bf16 v[168:171], a[232:235], v[44:47], v[168:171]\n"
      "ds_read_b128 a[224:227], v247 offset:12288\n"
      "v_fmac_f32 v188, v226, v217\n"
      "v_mfma_f32_16x16x32_bf16 v[176:179], a[236:239], v[44:47], v[176:179]\n"
      "v_fmac_f32 v189, v227, v217\n"
      "v_fmac_f32 v190, v228, v217\n"
      "v_mfma_f32_16x16x32_bf16 v[180:183], a[236:239], v[124:127], v[180:183]\n"
      "buffer_load_dwordx4 v[72:75], v254, s[12:15], s60 offen offset:2048 sc1\n"
      "v_fmac_f32 v191, v229, v217\n"
      "s_nop 1\n"
      "v_mfma_f32_16x16x32_bf16 v[188:191], a[240:243], v[124:127], v[188:191]\n"
      "ds_read_b128 v[226:229], v250 offset:256\n"
      "s_waitcnt lgkmcnt(5)\n"
      "v_mul_f32 v192, v222, v216\n"
      "v_mfma_f32_16x16x32_bf16 v[184:187], a[240:243], v[44:47], v[184:187]\n"
      "buffer_load_dwordx4 v[152:155], v254, s[12:15], s61 offen offset:2048 sc1\n"
      "v_mul_f32 v193, v223, v216\n"
      "s_waitcnt vmcnt(12) lgkmcnt(1)\n"
      "v_mfma_f32_16x16x32_bf16 v[160:163], a[212:215], v[48:51], v[160:163]\n"
      "ds_read_b128 a[228:231], v246 offset:13312\n"
      "v_mul_f32 v194, v224, v216\n"
      "v_mfma_f32_16x16x32_bf16 v[164:167], a[212:215], v[128:131], v[164:167]\n"
      "ds_read_b128 a[232:235], v246 offset:33792\n"
      "v_mul_f32 v195, v225, v216\n"
      "v_mfma_f32_16x16x32_bf16 v[172:175], a[216:219], v[128:131], v[172:175]\n"
      "ds_read_b128 a[236:239], v246 offset:54272\n"
      "v_mul_f32 v196, v222, v217\n"
      "v_mfma_f32_16x16x32_bf16 v[168:171], a[216:219], v[48:51], v[168:171]\n"
      "ds_read_b128 a[240:243], v247 offset:13312\n"
      "v_mul_f32 v197, v223, v217\n"
      "v_mfma_f32_16x16x32_bf16 v[176:179], a[220:223], v[48:51], v[176:179]\n"
      "v_mul_f32 v198, v224, v217\n"
      "v_mul_f32 v199, v225, v217\n"
      "v_mfma_f32_16x16x32_bf16 v[180:183], a[220:223], v[128:131], v[180:183]\n"
      "buffer_load_dwordx4 v[76:79], v254, s[12:15], s60 offen offset:3072 sc1\n"
      "ds_read_b128 v[222:225], v250 offset:320\n"
      "v_mfma_f32_16x16x32_bf16 v[188:191], a[224:227], v[128:131], v[188:191]\n"
      "s_waitcnt lgkmcnt(5)\n"
      "v_mul_f32 v200, v226, v216\n"
      "v_mfma_f32_16x16x32_bf16 v[184:187], a[224:227], v[48:51], v[184:187]\n"
      "buffer_load_dwordx4 v[156:159], v254, s[12:15], s61 offen offset:3072 sc1\n"
      "v_mul_f32 v201, v227, v216\n"
      "s_waitcnt vmcnt(12) lgkmcnt(1)\n"
      "v_mfma_f32_16x16x32_bf16 v[160:163], a[228:231], v[52:55], v[160:163]\n"
      "ds_read_b128 a[212:215], v246 offset:14336\n"
      "v_mul_f32 v202, v228, v216\n"
      "v_mfma_f32_16x16x32_bf16 v[164:167], a[228:231], v[132:135], v[164:167]\n"
      "ds_read_b128 a[216:219], v246 offset:34816\n"
      "v_mul_f32 v203, v229, v216\n"
      "v_mfma_f32_16x16x32_bf16 v[172:175], a[232:235], v[132:135], v[172:175]\n"
      "ds_read_b128 a[220:223], v246 offset:55296\n"
      "v_mul_f32 v204, v226, v217\n"
      "v_mfma_f32_16x16x32_bf16 v[168:171], a[232:235], v[52:55], v[168:171]\n"
      "ds_read_b128 a[224:227], v247 offset:14336\n"
      "v_mul_f32 v205, v227, v217\n"
      "v_mfma_f32_16x16x32_bf16 v[176:179], a[236:239], v[52:55], v[176:179]\n"
      "v_mul_f32 v206, v228, v217\n"
      "v_mul_f32 v207, v229, v217\n"
      "v_mfma_f32_16x16x32_bf16 v[180:183], a[236:239], v[132:135], v[180:183]\n"
      "buffer_load_dwordx4 a[128:131], v254, s[12:15], s59 offen offset:2048 sc1\n"
      "s_waitcnt lgkmcnt(4)\n"
      "v_mul_f32 v208, v222, v216\n"
      "v_mfma_f32_16x16x32_bf16 v[188:191], a[240:243], v[132:135], v[188:191]\n"
      "v_mul_f32 v209, v223, v216\n"
      "v_mul_f32 v210, v224, v216\n"
      "v_mfma_f32_16x16x32_bf16 v[184:187], a[240:243], v[52:55], v[184:187]\n"
      "buffer_load_dwordx4 a[132:135], v254, s[12:15], s59 offen offset:3072 sc1\n"
      "v_mul_f32 v211, v225, v216\n"
      "s_waitcnt vmcnt(12) lgkmcnt(0)\n"
      "v_mfma_f32_16x16x32_bf16 v[160:163], a[212:215], v[56:59], v[160:163]\n"
      "ds_read_b128 a[228:231], v246 offset:15360\n"
      "v_mul_f32 v212, v222, v217\n"
      "v_mfma_f32_16x16x32_bf16 v[164:167], a[212:215], v[136:139], v[164:167]\n"
      "ds_read_b128 a[232:235], v246 offset:35840\n"
      "v_mul_f32 v213, v223, v217\n"
      "v_mfma_f32_16x16x32_bf16 v[172:175], a[216:219], v[136:139], v[172:175]\n"
      "ds_read_b128 a[236:239], v246 offset:56320\n"
      "v_mul_f32 v214, v224, v217\n"
      "v_mfma_f32_16x16x32_bf16 v[168:171], a[216:219], v[56:59], v[168:171]\n"
      "ds_read_b128 a[240:243], v247 offset:15360\n"
      "v_mul_f32 v215, v225, v217\n"
      "v_mfma_f32_16x16x32_bf16 v[176:179], a[220:223], v[56:59], v[176:179]\n"
      "s_lshl2_add_u32 s96, s34, s3\n"
      "s_lshl_b32 s96, s96, 7\n"
      "v_mfma_f32_16x16x32_bf16 v[180:183], a[220:223], v[136:139], v[180:183]\n"
      "s_add_u32 s59, s59, 0x1000\n"
      "buffer_load_dwordx4 a[136:139], v254, s[12:15], s59 offen offset:0 sc1\n"
      "v_mfma_f32_16x16x32_bf16 v[188:191], a[224:227], v[136:139], v[188:191]\n"
      "s_add_u32 s96, s96, 0x80000\n"
      "s_add_u32 s97, s96, 0x8000\n"
      "v_mfma_f32_16x16x32_bf16 v[184:187], a[224:227], v[56:59], v[184:187]\n"
      "buffer_load_dwordx4 a[140:143], v254, s[12:15], s59 offen offset:1024 sc1\n"
      "s_add_u32 s62, s97, 0x8000\n"
      "s_waitcnt vmcnt(12) lgkmcnt(0)\n"
      "v_mfma_f32_16x16x32_bf16 v[160:163], a[228:231], v[60:63], v[160:163]\n"
      "ds_read_b128 a[212:215], v246 offset:16384\n"
      "s_lshl_b32 s43, s38, 5\n"
      "v_mfma_f32_16x16x32_bf16 v[164:167], a[228:231], v[140:143], v[164:167]\n"
      "ds_read_b128 a[216:219], v246 offset:36864\n"
      "s_add_u32 s43, s43, 0x80000\n"
      "v_mfma_f32_16x16x32_bf16 v[172:175], a[232:235], v[140:143], v[172:175]\n"
      "ds_read_b128 a[220:223], v246 offset:57344\n"
      "v_mov_b32 v220, s40\n"
      "v_mfma_f32_16x16x32_bf16 v[168:171], a[232:235], v[60:63], v[168:171]\n"
      "ds_read_b128 a[224:227], v247 offset:16384\n"
      "v_mov_b32 v221, s41\n"
      "v_mfma_f32_16x16x32_bf16 v[176:179], a[236:239], v[60:63], v[176:179]\n"
      "v_mov_b32 v222, s40\n"
      "v_mov_b32 v223, s41\n"
      "v_mfma_f32_16x16x32_bf16 v[180:183], a[236:239], v[140:143], v[180:183]\n"
      "buffer_load_dwordx4 a[144:147], v254, s[12:15], s59 offen offset:2048 sc1\n"
      "v_add_u32 v224, s42, v254\n"
      "v_mfma_f32_16x16x32_bf16 v[188:191], a[240:243], v[140:143], v[188:191]\n"
      "v_lshlrev_b32 v225, 1, v254\n"
      "v_add_u32 v228, s63, v249\n"
      "v_mfma_f32_16x16x32_bf16 v[184:187], a[240:243], v[60:63], v[184:187]\n"
      "buffer_load_dwordx4 a[148:151], v254, s[12:15], s59 offen offset:3072 sc1\n"
      "s_lshl_b32 s44, s3, 11\n"
      "s_waitcnt vmcnt(12) lgkmcnt(0)\n"
      "v_mfma_f32_16x16x32_bf16 v[160:163], a[212:215], v[64:67], v[160:163]\n"
      "ds_read_b128 a[228:231], v246 offset:17408\n"
      "s_sub_u32 s44, 49152, s44\n"
      "v_mfma_f32_16x16x32_bf16 v[164:167], a[212:215], v[144:147], v[164:167]\n"
      "ds_read_b128 a[232:235], v246 offset:37888\n"
      "v_add_u32 v229, s44, v251\n"
      "v_mfma_f32_16x16x32_bf16 v[172:175], a[216:219], v[144:147], v[172:175]\n"
      "ds_read_b128 a[236:239], v246 offset:58368\n"
      "v_mfma_f32_16x16x32_bf16 v[168:171], a[216:219], v[64:67], v[168:171]\n"
      "ds_read_b128 a[240:243], v247 offset:17408\n"
      "v_mfma_f32_16x16x32_bf16 v[176:179], a[220:223], v[64:67], v[176:179]\n"
      "v_mfma_f32_16x16x32_bf16 v[180:183], a[220:223], v[144:147], v[180:183]\n"
      "s_add_u32 s59, s59, 0x1000\n"
      "buffer_load_dwordx4 a[152:155], v254, s[12:15], s59 offen offset:0 sc1\n"
      "v_mfma_f32_16x16x32_bf16 v[188:191], a[224:227], v[144:147], v[188:191]\n"
      "v_mfma_f32_16x16x32_bf16 v[184:187], a[224:227], v[64:67], v[184:187]\n"
      "buffer_load_dwordx4 a[156:159], v254, s[12:15], s59 offen offset:1024 sc1\n"
      "s_waitcnt vmcnt(12) lgkmcnt(0)\n"
      "v_mfma_f32_16x16x32_bf16 v[160:163], a[228:231], v[68:71], v[160:163]\n"
      "ds_read_b128 a[212:215], v246 offset:18432\n"
      "v_mfma_f32_16x16x32_bf16 v[164:167], a[228:231], v[148:151], v[164:167]\n"
      "ds_read_b128 a[216:219], v246 offset:38912\n"
      "v_mfma_f32_16x16x32_bf16 v[172:175], a[232:235], v[148:151], v[172:175]\n"
      "ds_read_b128 a[220:223], v246 offset:59392\n"
      "v_mfma_f32_16x16x32_bf16 v[168:171], a[232:235], v[68:71], v[168:171]\n"
      "ds_read_b128 a[224:227], v247 offset:18432\n"
      "v_mfma_f32_16x16x32_bf16 v[176:179], a[236:239], v[68:71], v[176:179]\n"
      "v_mfma_f32_16x16x32_bf16 v[180:183], a[236:239], v[148:151], v[180:183]\n"
      "buffer_load_dwordx4 a[160:163], v254, s[12:15], s59 offen offset:2048 sc1\n"
      "v_mfma_f32_16x16x32_bf16 v[188:191], a[240:243], v[148:151], v[188:191]\n"
      "v_mfma_f32_16x16x32_bf16 v[184:187], a[240:243], v[68:71], v[184:187]\n"
      "buffer_load_dwordx4 a[164:167], v254, s[12:15], s59 offen offset:3072 sc1\n"
      "s_waitcnt vmcnt(12) lgkmcnt(0)\n"
      "v_mfma_f32_16x16x32_bf16 v[160:163], a[212:215], v[72:75], v[160:163]\n"
      "ds_read_b128 a[228:231], v246 offset:19456\n"
      "v_mfma_f32_16x16x32_bf16 v[164:167], a[212:215], v[152:155], v[164:167]\n"
      "ds_read_b128 a[232:235], v246 offset:39936\n"
      "v_mfma_f32_16x16x32_bf16 v[172:175], a[216:219], v[152:155], v[172:175]\n"
      "ds_read_b128 a[236:239], v246 offset:60416\n"
      "v_mfma_f32_16x16x32_bf16 v[168:171], a[216:219], v[72:75], v[168:171]\n"
      "ds_read_b128 a[240:243], v247 offset:19456\n"
      "v_mfma_f32_16x16x32_bf16 v[176:179], a[220:223], v[72:75], v[176:179]\n"
      "v_mfma_f32_16x16x32_bf16 v[180:183], a[220:223], v[152:155], v[180:183]\n"
      "s_add_u32 s59, s59, 0x1000\n"
      "buffer_load_dwordx4 a[168:171], v254, s[12:15], s59 offen offset:0 sc1\n"
      "v_mfma_f32_16x16x32_bf16 v[188:191], a[224:227], v[152:155], v[188:191]\n"
      "v_mfma_f32_16x16x32_bf16 v[184:187], a[224:227], v[72:75], v[184:187]\n"
      "buffer_load_dwordx4 a[172:175], v254, s[12:15], s59 offen offset:1024 sc1\n"
      "s_waitcnt vmcnt(12) lgkmcnt(0)\n"
      "v_mfma_f32_16x16x32_bf16 v[160:163], a[228:231], v[76:79], v[160:163]\n"
      "ds_read_b128 a[212:215], v247 offset:20480\n"
      "v_mfma_f32_16x16x32_bf16 v[164:167], a[228:231], v[156:159], v[164:167]\n"
      "ds_read_b128 a[216:219], v247 offset:21504\n"
      "v_mfma_f32_16x16x32_bf16 v[172:175], a[232:235], v[156:159], v[172:175]\n"
      "ds_read_b128 a[220:223], v247 offset:22528\n"
      "v_mfma_f32_16x16x32_bf16 v[168:171], a[232:235], v[76:79], v[168:171]\n"
      "ds_read_b128 a[224:227], v247 offset:23552\n"
      "v_mfma_f32_16x16x32_bf16 v[176:179], a[236:239], v[76:79], v[176:179]\n"
      "v_mfma_f32_16x16x32_bf16 v[180:183], a[236:239], v[156:159], v[180:183]\n"
      "buffer_load_dwordx4 a[176:179], v254, s[12:15], s59 offen offset:2048 sc1\n"
      "v_mfma_f32_16x16x32_bf16 v[188:191], a[240:243], v[156:159], v[188:191]\n"
      "v_mfma_f32_16x16x32_bf16 v[184:187], a[240:243], v[76:79], v[184:187]\n"
      "buffer_load_dwordx4 a[180:183], v254, s[12:15], s59 offen offset:3072 sc1\n"
      "s_waitcnt lgkmcnt(0)\n"
      "s_barrier\n"
      "v_mfma_f32_16x16x32_bf16 v[192:195], a[212:215], v[0:3], v[192:195]\n"
      "ds_read_b128 a[228:231], v247 offset:24576\n"
      "v_mul_f32 v160, v160, v218\n"
      "v_mul_f32 v161, v161, v218\n"
      "v_mul_f32 v162, v162, v218\n"
      "v_mul_f32 v163, v163, v218\n"
      "v_mul_f32 v164, v164, v219\n"
      "v_mfma_f32_16x16x32_bf16 v[196:199], a[212:215], v[80:83], v[196:199]\n"
      "ds_read_b128 a[232:235], v247 offset:25600\n"
      "v_mul_f32 v165, v165, v219\n"
      "v_mul_f32 v166, v166, v219\n"
      "v_mul_f32 v167, v167, v219\n"
      "v_exp_f32 v230, v160\n"
      "v_mfma_f32_16x16x32_bf16 v[192:195], a[216:219], v[4:7], v[192:195]\n"
      "ds_read_b128 a[236:239], v247 offset:26624\n"
      "v_exp_f32 v231, v161\n"
      "s_add_u32 s59, s59, 0x1000\n"
      "buffer_load_dwordx4 a[184:187], v254, s[12:15], s59 offen offset:0 sc1\n"
      "v_mfma_f32_16x16x32_bf16 v[196:199], a[216:219], v[84:87], v[196:199]\n"
      "ds_read_b128 a[240:243], v247 offset:27648\n"
      "v_exp_f32 v232, v162\n"
      "v_exp_f32 v233, v163\n"
      "v_mfma_f32_16x16x32_bf16 v[192:195], a[220:223], v[8:11], v[192:195]\n"
      "v_exp_f32 v234, v164\n"
      "v_exp_f32 v235, v165\n"
      "v_exp_f32 v236, v166\n"
      "v_mfma_f32_16x16x32_bf16 v[196:199], a[220:223], v[88:91], v[196:199]\n"
      "v_exp_f32 v237, v167\n"
      "v_fma_f32 v230, v230, s95, s95\n"
      "v_fma_f32 v231, v231, s95, s95\n"
      "v_fma_f32 v232, v232, s95, s95\n"
      "v_fma_f32 v233, v233, s95, s95\n"
      "v_mfma_f32_16x16x32_bf16 v[192:195], a[224:227], v[12:15], v[192:195]\n"
      "buffer_load_dwordx4 a[188:191], v254, s[12:15], s59 offen offset:1024 sc1\n"
      "v_fma_f32 v234, v234, s95, s95\n"
      "v_fma_f32 v235, v235, s95, s95\n"
      "v_fma_f32 v236, v236, s95, s95\n"
      "v_fma_f32 v237, v237, s95, s95\n"
      "v_mfma_f32_16x16x32_bf16 v[196:199], a[224:227], v[92:95], v[196:199]\n"
      "v_rcp_f32 v230, v230\n"
      "v_rcp_f32 v231, v231\n"
      "v_rcp_f32 v232, v232\n"
      "s_waitcnt lgkmcnt(0)\n"
      "v_mfma_f32_16x16x32_bf16 v[192:195], a[228:231], v[16:19], v[192:195]\n"
      "ds_read_b128 a[212:215], v247 offset:28672\n"
      "v_rcp_f32 v233, v233\n"
      "v_rcp_f32 v234, v234\n"
      "v_mfma_f32_16x16x32_bf16 v[196:199], a[228:231], v[96:99], v[196:199]\n"
      "ds_read_b128 a[216:219], v247 offset:29696\n"
      "v_rcp_f32 v235, v235\n"
      "buffer_load_dwordx4 a[192:195], v254, s[12:15], s59 offen offset:2048 sc1\n"
      "v_rcp_f32 v236, v236\n"
      "v_mfma_f32_16x16x32_bf16 v[192:195], a[232:235], v[20:23], v[192:195]\n"
      "ds_read_b128 a[220:223], v247 offset:30720\n"
      "v_rcp_f32 v237, v237\n"
      "v_mul_f32 v160, v160, v230\n"
      "v_mul_f32 v161, v161, v231\n"
      "v_mul_f32 v162, v162, v232\n"
      "v_mfma_f32_16x16x32_bf16 v[196:199], a[232:235], v[100:103], v[196:199]\n"
      "ds_read_b128 a[224:227], v247 offset:31744\n"
      "v_mul_f32 v163, v163, v233\n"
      "v_mul_f32 v164, v164, v234\n"
      "v_mul_f32 v165, v165, v235\n"
      "v_mul_f32 v166, v166, v236\n"
      "v_mul_f32 v167, v167, v237\n"
      "v_mfma_f32_16x16x32_bf16 v[192:195], a[236:239], v[24:27], v[192:195]\n"
      "buffer_load_dwordx4 a[196:199], v254, s[12:15], s59 offen offset:3072 sc1\n"
      "v_cvt_pk_bf16_f32 v238, v160, v161\n"
      "v_cvt_pk_bf16_f32 v240, v164, v165\n"
      "v_cvt_pk_bf16_f32 v239, v162, v163\n"
      "v_cvt_pk_bf16_f32 v241, v166, v167\n"
      "v_permlane16_swap_b32 v238, v240\n"
      "v_mfma_f32_16x16x32_bf16 v[196:199], a[236:239], v[104:107], v[196:199]\n"
      "v_permlane16_swap_b32 v239, v241\n"
      "buffer_store_dwordx4 v[238:241], v249, s[12:15], s70 offen offset:0 sc1\n"
      "v_mul_f32 v168, v168, v218\n"
      "v_mul_f32 v169, v169, v218\n"
      "v_mul_f32 v170, v170, v218\n"
      "v_mul_f32 v171, v171, v218\n"
      "v_mfma_f32_16x16x32_bf16 v[192:195], a[240:243], v[28:31], v[192:195]\n"
      "v_mul_f32 v172, v172, v219\n"
      "v_mul_f32 v173, v173, v219\n"
      "v_mul_f32 v174, v174, v219\n"
      "v_mul_f32 v175, v175, v219\n"
      "v_exp_f32 v230, v168\n"
      "v_mfma_f32_16x16x32_bf16 v[196:199], a[240:243], v[108:111], v[196:199]\n"
      "v_exp_f32 v231, v169\n"
      "s_add_u32 s59, s59, 0x10000\n"
      "buffer_load_dwordx4 a[200:203], v254, s[12:15], s59 offen offset:0 sc1\n"
      "v_exp_f32 v232, v170\n"
      "s_waitcnt lgkmcnt(0)\n"
      "v_mfma_f32_16x16x32_bf16 v[192:195], a[212:215], v[32:35], v[192:195]\n"
      "ds_read_b128 a[228:231], v247 offset:32768\n"
      "v_exp_f32 v233, v171\n"
      "v_exp_f32 v234, v172\n"
      "v_mfma_f32_16x16x32_bf16 v[196:199], a[212:215], v[112:115], v[196:199]\n"
      "ds_read_b128 a[232:235], v247 offset:33792\n"
      "v_exp_f32 v235, v173\n"
      "v_exp_f32 v236, v174\n"
      "v_mfma_f32_16x16x32_bf16 v[192:195], a[216:219], v[36:39], v[192:195]\n"
      "ds_read_b128 a[236:239], v247 offset:34816\n"
      ".Lk_pad5:\n"
      "s_cbranch_execz .Lk_hopret\n"
      "v_exp_f32 v237, v175\n"
      "v_fma_f32 v230, v230, s95, s95\n"
      "v_fma_f32 v231, v231, s95, s95\n"
      "v_fma_f32 v232, v232, s95, s95\n"
      "v_mfma_f32_16x16x32_bf16 v[196:199], a[216:219], v[116:119], v[196:199]\n"
      "ds_read_b128 a[240:243], v247 offset:35840\n"
      "v_fma_f32 v233, v233, s95, s95\n"
      "buffer_load_dwordx4 a[204:207], v254, s[12:15], s59 offen offset:1024 sc1\n"
      "v_fma_f32 v234, v234, s95, s95\n"
      "v_fma_f32 v235, v235, s95, s95\n"
      "v_fma_f32 v236, v236, s95, s95\n"
      "v_mfma_f32_16x16x32_bf16 v[192:195], a[220:223], v[40:43], v[192:195]\n"
      "v_fma_f32 v237, v237, s95, s95\n"
      "v_rcp_f32 v230, v230\n"
      "v_rcp_f32 v231, v231\n"
      "v_mfma_f32_16x16x32_bf16 v[196:199], a[220:223], v[120:123], v[196:199]\n"
      "v_rcp_f32 v232, v232\n"
      "v_rcp_f32 v233, v233\n"
      "v_rcp_f32 v234, v234\n"
      "v_mfma_f32_16x16x32_bf16 v[192:195], a[224:227], v[44:47], v[192:195]\n"
      "v_rcp_f32 v235, v235\n"
      "buffer_load_dwordx4 a[208:211], v254, s[12:15], s59 offen offset:2048 sc1\n"
      "v_rcp_f32 v236, v236\n"
      "v_mfma_f32_16x16x32_bf16 v[196:199], a[224:227], v[124:127], v[196:199]\n"
      "v_rcp_f32 v237, v237\n"
      "v_mul_f32 v168, v168, v230\n"
      "v_mul_f32 v169, v169, v231\n"
      "v_mul_f32 v170, v170, v232\n"
      "v_mul_f32 v171, v171, v233\n"
      "s_waitcnt lgkmcnt(0)\n"
      "v_mfma_f32_16x16x32_bf16 v[192:195], a[228:231], v[48:51], v[192:195]\n"
      "ds_read_b128 a[212:215], v247 offset:36864\n"
      "v_mul_f32 v172, v172, v234\n"
      "v_mul_f32 v173, v173, v235\n"
      "v_mul_f32 v174, v174, v236\n"
      "v_mul_f32 v175, v175, v237\n"
      "s_lshl2_add_u32 m0, s79, 1024\n"
      "s_nop 0\n"
      "buffer_load_dwordx4 v254, s[12:15], s59 offen offset:3072 sc1 lds\n"
      "v_mfma_f32_16x16x32_bf16 v[196:199], a[228:231], v[128:131], v[196:199]\n"
      "ds_read_b128 a[216:219], v247 offset:37888\n"
      "v_cvt_pk_bf16_f32 v242, v168, v169\n"
      "v_cvt_pk_bf16_f32 v244, v172, v173\n"
      "v_cvt_pk_bf16_f32 v243, v170, v171\n"
      "v_cvt_pk_bf16_f32 v245, v174, v175\n"
      "v_permlane16_swap_b32 v242, v244\n"
      "v_mfma_f32_16x16x32_bf16 v[192:195], a[232:235], v[52:55], v[192:195]\n"
      "ds_read_b128 a[220:223], v247 offset:38912\n"
      "v_permlane16_swap_b32 v243, v245\n"
      "buffer_store_dwordx4 v[242:245], v249, s[12:15], s70 offen offset:512 sc1\n"
      "v_mul_f32 v176, v176, v218\n"
      "v_mul_f32 v177, v177, v218\n"
      "v_mul_f32 v178, v178, v218\n"
      "v_mfma_f32_16x16x32_bf16 v[196:199], a[232:235], v[132:135], v[196:199]\n"
      "ds_read_b128 a[224:227], v247 offset:39936\n"
      "v_mul_f32 v179, v179, v218\n"
      "v_mul_f32 v180, v180, v219\n"
      "v_mul_f32 v181, v181, v219\n"
      "v_mul_f32 v182, v182, v219\n"
      "v_mul_f32 v183, v183, v219\n"
      "v_mfma_f32_16x16x32_bf16 v[192:195], a[236:239], v[56:59], v[192:195]\n"
      "v_exp_f32 v230, v176\n"
      "v_exp_f32 v231, v177\n"
      "s_add_u32 s59, s59, 0x1000\n"
      "s_lshl2_add_u32 m0, s79, 5120\n"
      "s_nop 0\n"
      "buffer_load_dwordx4 v254, s[12:15], s59 offen offset:0 sc1 lds\n"
      "v_mfma_f32_16x16x32_bf16 v[196:199], a[236:239], v[136:139], v[196:199]\n"
      "v_exp_f32 v232, v178\n"
      "v_exp_f32 v233, v179\n"
      "v_exp_f32 v234, v180\n"
      "v_mfma_f32_16x16x32_bf16 v[192:195], a[240:243], v[60:63], v[192:195]\n"
      "v_exp_f32 v235, v181\n"
      "v_exp_f32 v236, v182\n"
      "v_exp_f32 v237, v183\n"
      "v_mfma_f32_16x16x32_bf16 v[196:199], a[240:243], v[140:143], v[196:199]\n"
      "v_fma_f32 v230, v230, s95, s95\n"
      "v_fma_f32 v231, v231, s95, s95\n"
      "v_fma_f32 v232, v232, s95, s95\n"
      "v_fma_f32 v233, v233, s95, s95\n"
      "buffer_load_dwordx4 v254, s[12:15], s59 offen offset:1024 sc1 lds\n"
      "v_fma_f32 v234, v234, s95, s95\n"
      "s_waitcnt lgkmcnt(0)\n"
      "v_mfma_f32_16x16x32_bf16 v[192:195], a[212:215], v[64:67], v[192:195]\n"
      "ds_read_b128 a[228:231], v247 offset:40960\n"
      "v_fma_f32 v235, v235, s95, s95\n"
      "v_fma_f32 v236, v236, s95, s95\n"
      "v_fma_f32 v237, v237, s95, s95\n"
      "v_rcp_f32 v230, v230\n"
      "v_mfma_f32_16x16x32_bf16 v[196:199], a[212:215], v[144:147], v[196:199]\n"
      "ds_read_b128 a[232:235], v247 offset:41984\n"
      "v_rcp_f32 v231, v231\n"
      "v_rcp_f32 v232, v232\n"
      "v_mfma_f32_16x16x32_bf16 v[192:195], a[216:219], v[68:71], v[192:195]\n"
      "ds_read_b128 a[236:239], v247 offset:43008\n"
      "v_rcp_f32 v233, v233\n"
      "v_rcp_f32 v234, v234\n"
      "v_mfma_f32_16x16x32_bf16 v[196:199], a[216:219], v[148:151], v[196:199]\n"
      "ds_read_b128 a[240:243], v247 offset:44032\n"
      "v_rcp_f32 v235, v235\n"
      "buffer_load_dwordx4 v254, s[12:15], s59 offen offset:2048 sc1 lds\n"
      "v_rcp_f32 v236, v236\n"
      "v_mfma_f32_16x16x32_bf16 v[192:195], a[220:223], v[72:75], v[192:195]\n"
      "v_rcp_f32 v237, v237\n"
      "v_mul_f32 v176, v176, v230\n"
      "v_mul_f32 v177, v177, v231\n"
      "v_mul_f32 v178, v178, v232\n"
      "v_mul_f32 v179, v179, v233\n"
      "v_mfma_f32_16x16x32_bf16 v[196:199], a[220:223], v[152:155], v[196:199]\n"
      "v_mul_f32 v180, v180, v234\n"
      "v_mul_f32 v181, v181, v235\n"
      "v_mul_f32 v182, v182, v236\n"
      "v_mul_f32 v183, v183, v237\n"
      "buffer_load_dwordx4 v254, s[12:15], s59 offen offset:3072 sc1 lds\n"
      "v_cvt_pk_bf16_f32 v238, v176, v177\n"
      "v_mfma_f32_16x16x32_bf16 v[192:195], a[224:227], v[76:79], v[192:195]\n"
      "v_cvt_pk_bf16_f32 v240, v180, v181\n"
      "v_cvt_pk_bf16_f32 v239, v178, v179\n"
      "v_cvt_pk_bf16_f32 v241, v182, v183\n"
      "v_permlane16_swap_b32 v238, v240\n"
      "v_mfma_f32_16x16x32_bf16 v[196:199], a[224:227], v[156:159], v[196:199]\n"
      "v_permlane16_swap_b32 v239, v241\n"
      "buffer_store_dwordx4 v[238:241], v228, s[12:15], s73 offen offset:0 sc1\n"
      "v_mul_f32 v184, v184, v218\n"
      "v_mul_f32 v185, v185, v218\n"
      "v_mul_f32 v186, v186, v218\n"
      "v_mul_f32 v187, v187, v218\n"
      "s_waitcnt lgkmcnt(0)\n"
      "v_mfma_f32_16x16x32_bf16 v[200:203], a[228:231], v[0:3], v[200:203]\n"
      "ds_read_b128 a[212:215], v247 offset:45056\n"
      "v_mul_f32 v188, v188, v219\n"
      "v_mul_f32 v189, v189, v219\n"
      "v_mul_f32 v190, v190, v219\n"
      "v_mul_f32 v191, v191, v219\n"
      "v_mfma_f32_16x16x32_bf16 v[204:207], a[228:231], v[80:83], v[204:207]\n"
      "ds_read_b128 a[216:219], v247 offset:46080\n"
      "v_exp_f32 v230, v184\n"
      "v_exp_f32 v231, v185\n"
      "s_add_u32 s59, s59, 0x1000\n"
      "s_lshl2_add_u32 m0, s79, 9216\n"
      "s_nop 0\n"
      "buffer_load_dwordx4 v254, s[12:15], s59 offen offset:0 sc1 lds\n"
      "v_mfma_f32_16x16x32_bf16 v[200:203], a[232:235], v[4:7], v[200:203]\n"
      "ds_read_b128 a[220:223], v247 offset:47104\n"
      "v_exp_f32 v232, v186\n"
      "v_exp_f32 v233, v187\n"
      "v_mfma_f32_16x16x32_bf16 v[204:207], a[232:235], v[84:87], v[204:207]\n"
      "ds_read_b128 a[224:227], v247 offset:48128\n"
      "v_exp_f32 v234, v188\n"
      "v_exp_f32 v235, v189\n"
      "v_mfma_f32_16x16x32_bf16 v[200:203], a[236:239], v[8:11], v[200:203]\n"
      "v_exp_f32 v236, v190\n"
      "v_exp_f32 v237, v191\n"
      "v_fma_f32 v230, v230, s95, s95\n"
      "v_fma_f32 v231, v231, s95, s95\n"
      "v_mfma_f32_16x16x32_bf16 v[204:207], a[236:239], v[88:91], v[204:207]\n"
      "v_fma_f32 v232, v232, s95, s95\n"
      "v_fma_f32 v233, v233, s95, s95\n"
      "buffer_load_dwordx4 v254, s[12:15], s59 offen offset:1024 sc1 lds\n"
      "v_fma_f32 v234, v234, s95, s95\n"
      "v_fma_f32 v235, v235, s95, s95\n"
      "v_fma_f32 v236, v236, s95, s95\n"
      "v_mfma_f32_16x16x32_bf16 v[200:203], a[240:243], v[12:15], v[200:203]\n"
      "v_fma_f32 v237, v237, s95, s95\n"
      "v_rcp_f32 v230, v230\n"
      "v_rcp_f32 v231, v231\n"
      "v_mfma_f32_16x16x32_bf16 v[204:207], a[240:243], v[92:95], v[204:207]\n"
      "v_rcp_f32 v232, v232\n"
      "v_rcp_f32 v233, v233\n"
      "v_rcp_f32 v234, v234\n"
      "s_waitcnt lgkmcnt(0)\n"
      "v_mfma_f32_16x16x32_bf16 v[200:203], a[212:215], v[16:19], v[200:203]\n"
      "ds_read_b128 a[228:231], v247 offset:49152\n"
      "v_rcp_f32 v235, v235\n"
      "buffer_load_dwordx4 v254, s[12:15], s59 offen offset:2048 sc1 lds\n"
      "v_rcp_f32 v236, v236\n"
      "v_mfma_f32_16x16x32_bf16 v[204:207], a[212:215], v[96:99], v[204:207]\n"
      "ds_read_b128 a[232:235], v247 offset:50176\n"
      "v_rcp_f32 v237, v237\n"
      "v_mul_f32 v184, v184, v230\n"
      "v_mul_f32 v185, v185, v231\n"
      "v_mul_f32 v186, v186, v232\n"
      "v_mfma_f32_16x16x32_bf16 v[200:203], a[216:219], v[20:23], v[200:203]\n"
      "ds_read_b128 a[236:239], v247 offset:51200\n"
      "v_mul_f32 v187, v187, v233\n"
      "v_mul_f32 v188, v188, v234\n"
      "v_mul_f32 v189, v189, v235\n"
      "v_mul_f32 v190, v190, v236\n"
      "v_mul_f32 v191, v191, v237\n"
      "v_mfma_f32_16x16x32_bf16 v[204:207], a[216:219], v[100:103], v[204:207]\n"
      "ds_read_b128 a[240:243], v247 offset:52224\n"
      "buffer_load_dwordx4 v254, s[12:15], s59 offen offset:3072 sc1 lds\n"
      "v_cvt_pk_bf16_f32 v242, v184, v185\n"
      "v_cvt_pk_bf16_f32 v244, v188, v189\n"
      "v_cvt_pk_bf16_f32 v243, v186, v187\n"
      "v_cvt_pk_bf16_f32 v245, v190, v191\n"
      "v_mfma_f32_16x16x32_bf16 v[200:203], a[220:223], v[24:27], v[200:203]\n"
      "v_permlane16_swap_b32 v242, v244\n"
      "v_permlane16_swap_b32 v243, v245\n"
      "buffer_store_dwordx4 v[242:245], v249, s[12:15], s71 offen offset:0 sc1\n"
      "v_mul_f32 v192, v192, v218\n"
      "v_mul_f32 v193, v193, v218\n"
      "v_mul_f32 v194, v194, v218\n"
      "v_mfma_f32_16x16x32_bf16 v[204:207], a[220:223], v[104:107], v[204:207]\n"
      "v_mul_f32 v195, v195, v218\n"
      "v_mul_f32 v196, v196, v219\n"
      "v_mul_f32 v197, v197, v219\n"
      "v_mul_f32 v198, v198, v219\n"
      "v_mul_f32 v199, v199, v219\n"
      "v_mfma_f32_16x16x32_bf16 v[200:203], a[224:227], v[28:31], v[200:203]\n"
      "v_exp_f32 v230, v192\n"
      "v_exp_f32 v231, v193\n"
      "s_add_u32 s59, s59, 0x1000\n"
      "s_lshl2_add_u32 m0, s79, 13312\n"
      "s_nop 0\n"
      "buffer_load_dwordx4 v254, s[12:15], s59 offen offset:0 sc1 lds\n"
      "v_mfma_f32_16x16x32_bf16 v[204:207], a[224:227], v[108:111], v[204:207]\n"
      "v_exp_f32 v232, v194\n"
      "v_exp_f32 v233, v195\n"
      "v_exp_f32 v234, v196\n"
      "s_waitcnt lgkmcnt(0)\n"
      "v_mfma_f32_16x16x32_bf16 v[200:203], a[228:231], v[32:35], v[200:203]\n"
      "ds_read_b128 a[212:215], v247 offset:53248\n"
      "v_exp_f32 v235, v197\n"
      "v_exp_f32 v236, v198\n"
      "v_mfma_f32_16x16x32_bf16 v[204:207], a[228:231], v[112:115], v[204:207]\n"
      "ds_read_b128 a[216:219], v247 offset:54272\n"
      "v_exp_f32 v237, v199\n"
      "v_fma_f32 v230, v230, s95, s95\n"
      "v_fma_f32 v231, v231, s95, s95\n"
      "v_fma_f32 v232, v232, s95, s95\n"
      "v_mfma_f32_16x16x32_bf16 v[200:203], a[232:235], v[36:39], v[200:203]\n"
      "ds_read_b128 a[220:223], v247 offset:55296\n"
      "v_fma_f32 v233, v233, s95, s95\n"
      "buffer_load_dwordx4 v254, s[12:15], s59 offen offset:1024 sc1 lds\n"
      "v_fma_f32 v234, v234, s95, s95\n"
      "v_fma_f32 v235, v235, s95, s95\n"
      "v_fma_f32 v236, v236, s95, s95\n"
      "v_mfma_f32_16x16x32_bf16 v[204:207], a[232:235], v[116:119], v[204:207]\n"
      "ds_read_b128 a[224:227], v247 offset:56320\n"
      "v_fma_f32 v237, v237, s95, s95\n"
      "v_rcp_f32 v230, v230\n"
      "v_rcp_f32 v231, v231\n"
      "v_mfma_f32_16x16x32_bf16 v[200:203], a[236:239], v[40:43], v[200:203]\n"
      "v_rcp_f32 v232, v232\n"
      "v_rcp_f32 v233, v233\n"
      "v_rcp_f32 v234, v234\n"
      "v_mfma_f32_16x16x32_bf16 v[204:207], a[236:239], v[120:123], v[204:207]\n"
      "v_rcp_f32 v235, v235\n"
      "buffer_load_dwordx4 v254, s[12:15], s59 offen offset:2048 sc1 lds\n"
      "s_mov_b64 exec, 0x3\n"
      "s_waitcnt vmcnt(8)\n"
      "s_barrier\n"
      "buffer_store_dwordx4 v[220:223], v224, s[12:15], s43 offen sc1\n"
      "s_mov_b64 exec, -1\n"
      "s_add_u32 s43, s43, 0x8000\n"
      "v_mfma_f32_16x16x32_bf16 v[200:203], a[240:243], v[44:47], v[200:203]\n"
      "v_rcp_f32 v236, v236\n"
      "v_rcp_f32 v237, v237\n"
      "v_mul_f32 v192, v192, v230\n"
      "v_mul_f32 v193, v193, v231\n"
      "v_mfma_f32_16x16x32_bf16 v[204:207], a[240:243], v[124:127], v[204:207]\n"
      "v_mul_f32 v194, v194, v232\n"
      "v_mul_f32 v195, v195, v233\n"
      "v_mul_f32 v196, v196, v234\n"
      "v_mul_f32 v197, v197, v235\n"
      "v_mul_f32 v198, v198, v236\n"
      "buffer_load_dwordx4 v254, s[12:15], s59 offen offset:3072 sc1 lds\n"
      "s_waitcnt lgkmcnt(0)\n"
      "v_mfma_f32_16x16x32_bf16 v[200:203], a[212:215], v[48:51], v[200:203]\n"
      "ds_read_b128 a[228:231], v247 offset:57344\n"
      "v_mul_f32 v199, v199, v237\n"
      "v_cvt_pk_bf16_f32 v238, v192, v193\n"
      "v_cvt_pk_bf16_f32 v240, v196, v197\n"
      "v_cvt_pk_bf16_f32 v239, v194, v195\n"
      "v_cvt_pk_bf16_f32 v241, v198, v199\n"
      "v_mfma_f32_16x16x32_bf16 v[204:207], a[212:215], v[128:131], v[204:207]\n"
      "ds_read_b128 a[232:235], v247 offset:58368\n"
      "v_permlane16_swap_b32 v238, v240\n"
      "v_permlane16_swap_b32 v239, v241\n"
      "buffer_store_dwordx4 v[238:241], v249, s[12:15], s71 offen offset:512 sc1\n"
      "v_mfma_f32_16x16x32_bf16 v[200:203], a[216:219], v[52:55], v[200:203]\n"
      "ds_read_b128 a[236:239], v247 offset:59392\n"
      "v_mfma_f32_16x16x32_bf16 v[204:207], a[216:219], v[132:135], v[204:207]\n"
      "ds_read_b128 a[240:243], v247 offset:60416\n"
      "v_mfma_f32_16x16x32_bf16 v[200:203], a[220:223], v[56:59], v[200:203]\n"
      "v_mfma_f32_16x16x32_bf16 v[204:207], a[220:223], v[136:139], v[204:207]\n"
      "v_mfma_f32_16x16x32_bf16 v[200:203], a[224:227], v[60:63], v[200:203]\n"
      "v_mfma_f32_16x16x32_bf16 v[204:207], a[224:227], v[140:143], v[204:207]\n"
      "s_waitcnt lgkmcnt(0)\n"
      "v_mfma_f32_16x16x32_bf16 v[200:203], a[228:231], v[64:67], v[200:203]\n"
      "ds_read_b128 a[212:215], v248 offset:0\n"
      "v_mfma_f32_16x16x32_bf16 v[204:207], a[228:231], v[144:147], v[204:207]\n"
      "ds_read_b128 a[216:219], v248 offset:1024\n"
      "v_mfma_f32_16x16x32_bf16 v[200:203], a[232:235], v[68:71], v[200:203]\n"
      "ds_read_b128 a[220:223], v248 offset:2048\n"
      "v_mfma_f32_16x16x32_bf16 v[204:207], a[232:235], v[148:151], v[204:207]\n"
      "ds_read_b128 a[224:227], v248 offset:3072\n"
      "v_mfma_f32_16x16x32_bf16 v[200:203], a[236:239], v[72:75], v[200:203]\n"
      "v_mfma_f32_16x16x32_bf16 v[204:207], a[236:239], v[152:155], v[204:207]\n"
      "v_mfma_f32_16x16x32_bf16 v[200:203], a[240:243], v[76:79], v[200:203]\n"
      "v_mfma_f32_16x16x32_bf16 v[204:207], a[240:243], v[156:159], v[204:207]\n"
      "s_waitcnt lgkmcnt(0)\n"
      "v_mfma_f32_16x16x32_bf16 v[208:211], a[212:215], v[0:3], v[208:211]\n"
      "ds_read_b128 a[228:231], v248 offset:4096\n"
      "v_mfma_f32_16x16x32_bf16 v[212:215], a[212:215], v[80:83], v[212:215]\n"
      "ds_read_b128 a[232:235], v248 offset:5120\n"
      "v_mfma_f32_16x16x32_bf16 v[208:211], a[216:219], v[4:7], v[208:211]\n"
      "ds_read_b128 a[236:239], v248 offset:6144\n"
      "v_mul_f32 v200, v200, v218\n"
      "v_mul_f32 v201, v201, v218\n"
      "v_mul_f32 v202, v202, v218\n"
      "v_mul_f32 v203, v203, v218\n"
      "v_mul_f32 v204, v204, v219\n"
      "v_mfma_f32_16x16x32_bf16 v[212:215], a[216:219], v[84:87], v[212:215]\n"
      "ds_read_b128 a[240:243], v248 offset:7168\n"
      "v_mul_f32 v205, v205, v219\n"
      "v_mul_f32 v206, v206, v219\n"
      "v_mul_f32 v207, v207, v219\n"
      "v_exp_f32 v230, v200\n"
      "v_mfma_f32_16x16x32_bf16 v[208:211], a[220:223], v[8:11], v[208:211]\n"
      "v_exp_f32 v231, v201\n"
      "s_add_u32 s59, s59, 0x1000\n"
      "s_lshl2_add_u32 m0, s79, 17408\n"
      "s_nop 0\n"
      "buffer_load_dwordx4 v254, s[12:15], s59 offen offset:0 sc1 lds\n"
      "v_mfma_f32_16x16x32_bf16 v[212:215], a[220:223], v[88:91], v[212:215]\n"
      "v_exp_f32 v232, v202\n"
      "v_exp_f32 v233, v203\n"
      "v_exp_f32 v234, v204\n"
      "v_mfma_f32_16x16x32_bf16 v[208:211], a[224:227], v[12:15], v[208:211]\n"
      "v_exp_f32 v235, v205\n"
      "v_exp_f32 v236, v206\n"
      "v_exp_f32 v237, v207\n"
      "v_mfma_f32_16x16x32_bf16 v[212:215], a[224:227], v[92:95], v[212:215]\n"
      "v_fma_f32 v230, v230, s95, s95\n"
      "v_fma_f32 v231, v231, s95, s95\n"
      "v_fma_f32 v232, v232, s95, s95\n"
      "v_fma_f32 v233, v233, s95, s95\n"
      "buffer_load_dwordx4 v254, s[12:15], s59 offen offset:1024 sc1 lds\n"
      "v_fma_f32 v234, v234, s95, s95\n"
      "s_waitcnt lgkmcnt(0)\n"
      "v_mfma_f32_16x16x32_bf16 v[208:211], a[228:231], v[16:19], v[208:211]\n"
      "ds_read_b128 a[212:215], v248 offset:8192\n"
      "v_fma_f32 v235, v235, s95, s95\n"
      "v_fma_f32 v236, v236, s95, s95\n"
      "v_fma_f32 v237, v237, s95, s95\n"
      "v_rcp_f32 v230, v230\n"
      "v_mfma_f32_16x16x32_bf16 v[212:215], a[228:231], v[96:99], v[212:215]\n"
      "ds_read_b128 a[216:219], v248 offset:9216\n"
      "v_rcp_f32 v231, v231\n"
      "v_rcp_f32 v232, v232\n"
      "v_mfma_f32_16x16x32_bf16 v[208:211], a[232:235], v[20:23], v[208:211]\n"
      "ds_read_b128 a[220:223], v248 offset:10240\n"
      "v_rcp_f32 v233, v233\n"
      "v_rcp_f32 v234, v234\n"
      "v_mfma_f32_16x16x32_bf16 v[212:215], a[232:235], v[100:103], v[212:215]\n"
      "ds_read_b128 a[224:227], v248 offset:11264\n"
      "v_rcp_f32 v235, v235\n"
      "buffer_load_dwordx4 v254, s[12:15], s59 offen offset:2048 sc1 lds\n"
      "s_mov_b64 exec, 0xf\n"
      "buffer_load_dwordx2 v[226:227], v225, s[12:15], s96 offen sc1\n"
      "s_mov_b64 exec, -1\n"
      "v_mfma_f32_16x16x32_bf16 v[208:211], a[236:239], v[24:27], v[208:211]\n"
      "v_rcp_f32 v236, v236\n"
      "v_rcp_f32 v237, v237\n"
      "v_mul_f32 v200, v200, v230\n"
      "v_mul_f32 v201, v201, v231\n"
      "v_mfma_f32_16x16x32_bf16 v[212:215], a[236:239], v[104:107], v[212:215]\n"
      "v_mul_f32 v202, v202, v232\n"
      "v_mul_f32 v203, v203, v233\n"
      "v_mul_f32 v204, v204, v234\n"
      "v_mul_f32 v205, v205, v235\n"
      "v_mul_f32 v206, v206, v236\n"
      "buffer_load_dwordx4 v254, s[12:15], s59 offen offset:3072 sc1 lds\n"
      "v_mfma_f32_16x16x32_bf16 v[208:211], a[240:243], v[28:31], v[208:211]\n"
      "v_mul_f32 v207, v207, v237\n"
      "v_cvt_pk_bf16_f32 v242, v200, v201\n"
      "v_cvt_pk_bf16_f32 v244, v204, v205\n"
      "v_cvt_pk_bf16_f32 v243, v202, v203\n"
      "v_cvt_pk_bf16_f32 v245, v206, v207\n"
      "v_permlane16_swap_b32 v242, v244\n"
      "v_mfma_f32_16x16x32_bf16 v[212:215], a[240:243], v[108:111], v[212:215]\n"
      "v_permlane16_swap_b32 v243, v245\n"
      "buffer_store_dwordx4 v[242:245], v249, s[12:15], s72 offen offset:0 sc1\n"
      "s_waitcnt lgkmcnt(0)\n"
      "v_mfma_f32_16x16x32_bf16 v[208:211], a[212:215], v[32:35], v[208:211]\n"
      "ds_read_b128 a[228:231], v248 offset:12288\n"
      "v_mfma_f32_16x16x32_bf16 v[212:215], a[212:215], v[112:115], v[212:215]\n"
      "ds_read_b128 a[232:235], v248 offset:13312\n"
      "v_mfma_f32_16x16x32_bf16 v[208:211], a[216:219], v[36:39], v[208:211]\n"
      "ds_read_b128 a[236:239], v248 offset:14336\n"
      "v_mfma_f32_16x16x32_bf16 v[212:215], a[216:219], v[116:119], v[212:215]\n"
      "ds_read_b128 a[240:243], v248 offset:15360\n"
      "v_mfma_f32_16x16x32_bf16 v[208:211], a[220:223], v[40:43], v[208:211]\n"
      "v_mfma_f32_16x16x32_bf16 v[212:215], a[220:223], v[120:123], v[212:215]\n"
      "v_mfma_f32_16x16x32_bf16 v[208:211], a[224:227], v[44:47], v[208:211]\n"
      "v_mfma_f32_16x16x32_bf16 v[212:215], a[224:227], v[124:127], v[212:215]\n"
      "s_waitcnt lgkmcnt(0)\n"
      "v_mfma_f32_16x16x32_bf16 v[208:211], a[228:231], v[48:51], v[208:211]\n"
      "ds_read_b128 a[212:215], v248 offset:16384\n"
      "v_mfma_f32_16x16x32_bf16 v[212:215], a[228:231], v[128:131], v[212:215]\n"
      "ds_read_b128 a[216:219], v248 offset:17408\n"
      "v_mfma_f32_16x16x32_bf16 v[208:211], a[232:235], v[52:55], v[208:211]\n"
      "ds_read_b128 a[220:223], v248 offset:18432\n"
      "v_mfma_f32_16x16x32_bf16 v[212:215], a[232:235], v[132:135], v[212:215]\n"
      "ds_read_b128 a[224:227], v248 offset:19456\n"
      "v_mfma_f32_16x16x32_bf16 v[208:211], a[236:239], v[56:59], v[208:211]\n"
      "v_mfma_f32_16x16x32_bf16 v[212:215], a[236:239], v[136:139], v[212:215]\n"
      "v_mfma_f32_16x16x32_bf16 v[208:211], a[240:243], v[60:63], v[208:211]\n"
      "v_mfma_f32_16x16x32_bf16 v[212:215], a[240:243], v[140:143], v[212:215]\n"
      "s_waitcnt lgkmcnt(0)\n"
      "v_mfma_f32_16x16x32_bf16 v[208:211], a[212:215], v[64:67], v[208:211]\n"
      "v_mfma_f32_16x16x32_bf16 v[212:215], a[212:215], v[144:147], v[212:215]\n"
      "v_mfma_f32_16x16x32_bf16 v[208:211], a[216:219], v[68:71], v[208:211]\n"
      "v_mfma_f32_16x16x32_bf16 v[212:215], a[216:219], v[148:151], v[212:215]\n"
      "v_mfma_f32_16x16x32_bf16 v[208:211], a[220:223], v[72:75], v[208:211]\n"
      "v_mfma_f32_16x16x32_bf16 v[212:215], a[220:223], v[152:155], v[212:215]\n"
      "v_mfma_f32_16x16x32_bf16 v[208:211], a[224:227], v[76:79], v[208:211]\n"
      "v_mfma_f32_16x16x32_bf16 v[212:215], a[224:227], v[156:159], v[212:215]\n"
      "s_waitcnt vmcnt(2)\n"
      "v_cmp_ne_u64 vcc, s[40:41], v[226:227]\n"
      "s_and_b32 vcc_lo, vcc_lo, 0xf\n"
      "s_cbranch_scc0 .Lk_ok_12\n"
      "s_mov_b64 exec, 0xf\n"
      ".Lk_poll_13:\n"
      "s_sleep 1\n"
      "buffer_load_dwordx2 v[226:227], v225, s[12:15], s96 offen sc1\n"
      "s_waitcnt vmcnt(0)\n"
      "v_cmp_ne_u64 vcc, s[40:41], v[226:227]\n"
      "s_cbranch_vccz .Lk_polld_14\n"
      "s_cmp_eq_u32 s39, 0\n"
      "s_cbranch_scc1 .Lk_polld_14\n"
      "s_sub_u32 s39, s39, 1\n"
      "s_branch .Lk_poll_13\n"
      ".Lk_polld_14:\n"
      "s_mov_b64 exec, -1\n"
      ".Lk_ok_12:\n"
      "s_nop 2\n"
      "v_mul_f32 v208, v208, v218\n"
      "v_mul_f32 v209, v209, v218\n"
      "v_mul_f32 v210, v210, v218\n"
      "v_mul_f32 v211, v211, v218\n"
      "v_mul_f32 v212, v212, v219\n"
      "buffer_load_dwordx4 v[0:3], v254, s[12:15], s64 offen offset:0 sc1\n"
      "v_mul_f32 v213, v213, v219\n"
      "v_mul_f32 v214, v214, v219\n"
      "v_mul_f32 v215, v215, v219\n"
      "v_exp_f32 v230, v208\n"
      "v_exp_f32 v231, v209\n"
      "buffer_load_dwordx4 v[4:7], v254, s[12:15], s64 offen offset:1024 sc1\n"
      "v_exp_f32 v232, v210\n"
      "v_exp_f32 v233, v211\n"
      "v_exp_f32 v234, v212\n"
      "v_exp_f32 v235, v213\n"
      "v_exp_f32 v236, v214\n"
      "buffer_load_dwordx4 v[8:11], v254, s[12:15], s64 offen offset:2048 sc1\n"
      "v_exp_f32 v237, v215\n"
      "v_fma_f32 v230, v230, s95, s95\n"
      "v_fma_f32 v231, v231, s95, s95\n"
      "v_fma_f32 v232, v232, s95, s95\n"
      "v_fma_f32 v233, v233, s95, s95\n"
      "buffer_load_dwordx4 v[12:15], v254, s[12:15], s64 offen offset:3072 sc1\n"
      "v_fma_f32 v234, v234, s95, s95\n"
      "v_fma_f32 v235, v235, s95, s95\n"
      "v_fma_f32 v236, v236, s95, s95\n"
      "v_fma_f32 v237, v237, s95, s95\n"
      "v_rcp_f32 v230, v230\n"
      "buffer_load_dwordx4 v[16:19], v229, s[12:15], s64 offen offset:0 sc1\n"
      "v_rcp_f32 v231, v231\n"
      "v_rcp_f32 v232, v232\n"
      "v_rcp_f32 v233, v233\n"
      "v_rcp_f32 v234, v234\n"
      "v_rcp_f32 v235, v235\n"
      "buffer_load_dwordx4 v[20:23], v229, s[12:15], s64 offen offset:1024 sc1\n"
      "s_mov_b64 exec, 0x3\n"
      "s_barrier\n"
      "buffer_store_dwordx4 v[220:223], v224, s[12:15], s43 offen sc1\n"
      "s_mov_b64 exec, -1\n"
      "s_add_u32 s43, s43, 0x8000\n"
      "v_rcp_f32 v236, v236\n"
      "v_rcp_f32 v237, v237\n"
      "v_mul_f32 v208, v208, v230\n"
      "v_mul_f32 v209, v209, v231\n"
      "buffer_load_dwordx4 v[24:27], v254, s[12:15], s65 offen offset:0 sc1\n"
      "v_mul_f32 v210, v210, v232\n"
      "v_mul_f32 v211, v211, v233\n"
      "v_mul_f32 v212, v212, v234\n"
      "v_mul_f32 v213, v213, v235\n"
      "v_mul_f32 v214, v214, v236\n"
      "buffer_load_dwordx4 v[28:31], v254, s[12:15], s65 offen offset:1024 sc1\n"
      "v_mul_f32 v215, v215, v237\n"
      "v_cvt_pk_bf16_f32 v238, v208, v209\n"
      "v_cvt_pk_bf16_f32 v240, v212, v213\n"
      "v_cvt_pk_bf16_f32 v239, v210, v211\n"
      "v_cvt_pk_bf16_f32 v241, v214, v215\n"
      "buffer_load_dwordx4 v[32:35], v254, s[12:15], s65 offen offset:2048 sc1\n"
      "v_permlane16_swap_b32 v238, v240\n"
      "v_permlane16_swap_b32 v239, v241\n"
      "buffer_store_dwordx4 v[238:241], v249, s[12:15], s72 offen offset:512 sc1\n"
      "buffer_load_dwordx4 v[36:39], v254, s[12:15], s65 offen offset:3072 sc1\n"
      "s_waitcnt vmcnt(11)\n"
      "v_mfma_f32_16x16x32_bf16 v[92:95], a[0:3], v[0:3], 0\n"
      "v_accvgpr_read_b32 v192, a244\n"
      "v_accvgpr_read_b32 v193, a245\n"
      "v_mfma_f32_16x16x32_bf16 v[96:99], a[4:7], v[0:3], 0\n"
      "v_accvgpr_read_b32 v194, a246\n"
      "v_accvgpr_read_b32 v195, a247\n"
      "v_mfma_f32_16x16x32_bf16 v[100:103], a[8:11], v[0:3], 0\n"
      "v_accvgpr_read_b32 v196, a248\n"
      "v_accvgpr_read_b32 v197, a249\n"
      "v_mfma_f32_16x16x32_bf16 v[104:107], a[12:15], v[0:3], 0\n"
      "v_accvgpr_read_b32 v198, a250\n"
      "v_accvgpr_read_b32 v199, a251\n"
      "v_mfma_f32_16x16x32_bf16 v[108:111], a[16:19], v[0:3], 0\n"
      "buffer_load_dwordx4 v[0:3], v229, s[12:15], s65 offen offset:0 sc1\n"
      "s_waitcnt vmcnt(11)\n"
      "v_mfma_f32_16x16x32_bf16 v[92:95], a[20:23], v[4:7], v[92:95]\n"
      "v_accvgpr_read_b32 v200, a252\n"
      "v_accvgpr_read_b32 v201, a253\n"
      "v_mfma_f32_16x16x32_bf16 v[96:99], a[24:27], v[4:7], v[96:99]\n"
      "v_add_u32 v207, 16384, v254\n"
      "v_add_u32 v208, 32768, v254\n"
      "v_mfma_f32_16x16x32_bf16 v[100:103], a[28:31], v[4:7], v[100:103]\n"
      "s_lshl2_add_u32 s44, s79, 4096\n"
      "v_add_u32 v155, s44, v254\n"
      "v_mfma_f32_16x16x32_bf16 v[104:107], a[32:35], v[4:7], v[104:107]\n"
      "s_cmp_eq_u32 s2, 0\n"
      "s_cselect_b32 s44, s42, 0x40000000\n"
      "v_mfma_f32_16x16x32_bf16 v[108:111], a[36:39], v[4:7], v[108:111]\n"
      "buffer_load_dwordx4 v[4:7], v229, s[12:15], s65 offen offset:1024 sc1\n"
      "s_waitcnt vmcnt(11)\n"
      "v_mfma_f32_16x16x32_bf16 v[92:95], a[40:43], v[8:11], v[92:95]\n"
      "v_mov_b32 v159, s44\n"
      "v_lshlrev_b32 v160, 16, v192\n"
      "v_mfma_f32_16x16x32_bf16 v[96:99], a[44:47], v[8:11], v[96:99]\n"
      "v_and_b32 v161, 0xffff0000, v192\n"
      "v_lshlrev_b32 v162, 16, v193\n"
      "v_mfma_f32_16x16x32_bf16 v[100:103], a[48:51], v[8:11], v[100:103]\n"
      "v_and_b32 v163, 0xffff0000, v193\n"
      "v_lshlrev_b32 v164, 16, v194\n"
      "v_mfma_f32_16x16x32_bf16 v[104:107], a[52:55], v[8:11], v[104:107]\n"
      "v_and_b32 v165, 0xffff0000, v194\n"
      "v_lshlrev_b32 v166, 16, v195\n"
      "v_mfma_f32_16x16x32_bf16 v[108:111], a[56:59], v[8:11], v[108:111]\n"
      "buffer_load_dwordx4 v[8:11], v254, s[12:15], s66 offen offset:0 sc1\n"
      "s_waitcnt vmcnt(11)\n"
      "v_mfma_f32_16x16x32_bf16 v[92:95], a[60:63], v[12:15], v[92:95]\n"
      "v_and_b32 v167, 0xffff0000, v195\n"
      "v_lshlrev_b32 v168, 16, v196\n"
      "v_mfma_f32_16x16x32_bf16 v[96:99], a[64:67], v[12:15], v[96:99]\n"
      "v_and_b32 v169, 0xffff0000, v196\n"
      "v_lshlrev_b32 v170, 16, v197\n"
      "v_mfma_f32_16x16x32_bf16 v[100:103], a[68:71], v[12:15], v[100:103]\n"
      "v_and_b32 v171, 0xffff0000, v197\n"
      "v_lshlrev_b32 v172, 16, v198\n"
      "v_mfma_f32_16x16x32_bf16 v[104:107], a[72:75], v[12:15], v[104:107]\n"
      "v_and_b32 v173, 0xffff0000, v198\n"
      "v_lshlrev_b32 v174, 16, v199\n"
      "v_mfma_f32_16x16x32_bf16 v[108:111], a[76:79], v[12:15], v[108:111]\n"
      "buffer_load_dwordx4 v[12:15], v254, s[12:15], s66 offen offset:1024 sc1\n"
      "s_waitcnt vmcnt(11)\n"
      "v_mfma_f32_16x16x32_bf16 v[92:95], a[80:83], v[16:19], v[92:95]\n"
      "v_and_b32 v175, 0xffff0000, v199\n"
      "v_lshlrev_b32 v176, 16, v200\n"
      "v_mfma_f32_16x16x32_bf16 v[96:99], a[84:87], v[16:19], v[96:99]\n"
      "v_and_b32 v177, 0xffff0000, v200\n"
      "v_lshlrev_b32 v178, 16, v201\n"
      "v_mfma_f32_16x16x32_bf16 v[100:103], a[88:91], v[16:19], v[100:103]\n"
      "v_and_b32 v179, 0xffff0000, v201\n"
      "s_add_u32 s44, s3, 1\n"
      "v_mfma_f32_16x16x32_bf16 v[104:107], a[92:95], v[16:19], v[104:107]\n"
      "s_and_b32 s44, s44, 3\n"
      "s_mul_i32 s44, s44, 15360\n"
      "v_mfma_f32_16x16x32_bf16 v[108:111], a[96:99], v[16:19], v[108:111]\n"
      "buffer_load_dwordx4 v[16:19], v254, s[12:15], s66 offen offset:2048 sc1\n"
      "s_waitcnt vmcnt(11)\n"
      "v_mfma_f32_16x16x32_bf16 v[92:95], a[100:103], v[20:23], v[92:95]\n"
      "s_add_u32 s44, s44, 102400\n"
      "v_add_u32 v202, s44, v254\n"
      "v_mfma_f32_16x16x32_bf16 v[96:99], a[104:107], v[20:23], v[96:99]\n"
      "s_add_u32 s44, s3, 2\n"
      "s_and_b32 s44, s44, 3\n"
      "v_mfma_f32_16x16x32_bf16 v[100:103], a[108:111], v[20:23], v[100:103]\n"
      "s_mul_i32 s44, s44, 15360\n"
      "s_add_u32 s44, s44, 107520\n"
      "v_mfma_f32_16x16x32_bf16 v[104:107], a[112:115], v[20:23], v[104:107]\n"
      "v_add_u32 v203, s44, v254\n"
      "s_add_u32 s44, s3, 3\n"
      "v_mfma_f32_16x16x32_bf16 v[108:111], a[116:119], v[20:23], v[108:111]\n"
      "buffer_load_dwordx4 v[20:23], v254, s[12:15], s66 offen offset:3072 sc1\n"
      "s_waitcnt vmcnt(10)\n"
      "v_mfma_f32_16x16x32_bf16 v[112:115], a[0:3], v[24:27], 0\n"
      "s_and_b32 s44, s44, 3\n"
      "s_mul_i32 s44, s44, 15360\n"
      "v_mfma_f32_16x16x32_bf16 v[116:119], a[4:7], v[24:27], 0\n"
      "s_add_u32 s44, s44, 112640\n"
      "v_add_u32 v204, s44, v254\n"
      "v_mfma_f32_16x16x32_bf16 v[120:123], a[8:11], v[24:27], 0\n"
      "s_mul_i32 s44, s3, 15360\n"
      "s_add_u32 s44, s44, 102400\n"
      "v_mfma_f32_16x16x32_bf16 v[124:127], a[12:15], v[24:27], 0\n"
      "v_add_u32 v205, s44, v254\n"
      "s_lshl2_add_u32 s44, s37, s3\n"
      "v_mfma_f32_16x16x32_bf16 v[128:131], a[16:19], v[24:27], 0\n"
      "s_mov_b64 exec, 0xf\n"
      "buffer_load_dwordx2 v[226:227], v225, s[12:15], s97 offen sc1\n"
      "s_mov_b64 exec, -1\n"
      "buffer_load_dwordx4 v[24:27], v229, s[12:15], s66 offen offset:0 sc1\n"
      "s_waitcnt vmcnt(11)\n"
      "v_mfma_f32_16x16x32_bf16 v[112:115], a[20:23], v[28:31], v[112:115]\n"
      "s_mul_i32 s92, s44, 20480\n"
      "s_mul_i32 s45, s36, 160\n"
      "v_mfma_f32_16x16x32_bf16 v[116:119], a[24:27], v[28:31], v[116:119]\n"
      "s_add_u32 s92, s92, s45\n"
      "s_mul_i32 s44, s3, 2560\n"
      "v_mfma_f32_16x16x32_bf16 v[120:123], a[28:31], v[28:31], v[120:123]\n"
      "s_add_u32 s44, s44, 90112\n"
      "v_mul_u32_u24 v152, 0xa0, v252\n"
      "v_mfma_f32_16x16x32_bf16 v[124:127], a[32:35], v[28:31], v[124:127]\n"
      "v_add_u32 v152, s44, v152\n"
      "v_lshl_add_u32 v153, v253, 4, v152\n"
      "v_mfma_f32_16x16x32_bf16 v[128:131], a[36:39], v[28:31], v[128:131]\n"
      "buffer_load_dwordx4 v[28:31], v229, s[12:15], s66 offen offset:1024 sc1\n"
      "s_waitcnt vmcnt(11)\n"
      "v_mfma_f32_16x16x32_bf16 v[112:115], a[40:43], v[32:35], v[112:115]\n"
      "v_lshl_add_u32 v154, v253, 3, v152\n"
      "v_add_u32 v152, s44, v254\n"
      "v_mfma_f32_16x16x32_bf16 v[116:119], a[44:47], v[32:35], v[116:119]\n"
      "v_lshrrev_b32 v180, 4, v254\n"
      "v_add_u32 v181, 0, v180\n"
      "v_mfma_f32_16x16x32_bf16 v[120:123], a[48:51], v[32:35], v[120:123]\n"
      "v_mul_u32_u24 v182, 0x199a, v181\n"
      "v_lshrrev_b32 v182, 16, v182\n"
      "v_mfma_f32_16x16x32_bf16 v[124:127], a[52:55], v[32:35], v[124:127]\n"
      "v_mul_u32_u24 v182, 0x460, v182\n"
      "v_lshl_add_u32 v156, v181, 4, v182\n"
      "v_mfma_f32_16x16x32_bf16 v[128:131], a[56:59], v[32:35], v[128:131]\n"
      "buffer_load_dwordx4 v[32:35], v254, s[12:15], s67 offen offset:0 sc1\n"
      "s_waitcnt vmcnt(10)\n"
      "v_mfma_f32_16x16x32_bf16 v[112:115], a[60:63], v[36:39], v[112:115]\n"
      "v_add_u32 v181, 64, v180\n"
      "v_mul_u32_u24 v182, 0x199a, v181\n"
      "v_mfma_f32_16x16x32_bf16 v[116:119], a[64:67], v[36:39], v[116:119]\n"
      "v_lshrrev_b32 v182, 16, v182\n"
      "v_mul_u32_u24 v182, 0x460, v182\n"
      "v_mfma_f32_16x16x32_bf16 v[120:123], a[68:71], v[36:39], v[120:123]\n"
      "s_mov_b64 exec, 0x3\n"
      "s_barrier\n"
      "buffer_store_dwordx4 v[220:223], v224, s[12:15], s43 offen sc1\n"
      "s_mov_b64 exec, -1\n"
      "v_mfma_f32_16x16x32_bf16 v[124:127], a[72:75], v[36:39], v[124:127]\n"
      "v_lshl_add_u32 v157, v181, 4, v182\n"
      "s_nop 0\n"
      ".Lk_pad6:\n"
      "s_cbranch_execz .Lk_hopret\n"
      "v_add_u32 v181, 128, v180\n"
      "v_mfma_f32_16x16x32_bf16 v[128:131], a[76:79], v[36:39], v[128:131]\n"
      "buffer_load_dwordx4 v[36:39], v254, s[12:15], s67 offen offset:1024 sc1\n"
      "s_waitcnt vmcnt(11)\n"
      "v_mfma_f32_16x16x32_bf16 v[112:115], a[80:83], v[0:3], v[112:115]\n"
      "v_mul_u32_u24 v182, 0x199a, v181\n"
      "v_lshrrev_b32 v182, 16, v182\n"
      "v_mfma_f32_16x16x32_bf16 v[116:119], a[84:87], v[0:3], v[116:119]\n"
      "v_mul_u32_u24 v182, 0x460, v182\n"
      "v_lshl_add_u32 v158, v181, 4, v182\n"
      "v_mfma_f32_16x16x32_bf16 v[120:123], a[88:91], v[0:3], v[120:123]\n"
      "v_mfma_f32_16x16x32_bf16 v[124:127], a[92:95], v[0:3], v[124:127]\n"
      "v_mfma_f32_16x16x32_bf16 v[128:131], a[96:99], v[0:3], v[128:131]\n"
      "buffer_load_dwordx4 v[0:3], v254, s[12:15], s67 offen offset:2048 sc1\n"
      "s_waitcnt vmcnt(11)\n"
      "v_mfma_f32_16x16x32_bf16 v[112:115], a[100:103], v[4:7], v[112:115]\n"
      "v_mfma_f32_16x16x32_bf16 v[116:119], a[104:107], v[4:7], v[116:119]\n"
      "v_mfma_f32_16x16x32_bf16 v[120:123], a[108:111], v[4:7], v[120:123]\n"
      "v_mfma_f32_16x16x32_bf16 v[124:127], a[112:115], v[4:7], v[124:127]\n"
      "v_mfma_f32_16x16x32_bf16 v[128:131], a[116:119], v[4:7], v[128:131]\n"
      "buffer_load_dwordx4 v[4:7], v254, s[12:15], s67 offen offset:3072 sc1\n"
      "s_waitcnt vmcnt(11)\n"
      "v_mfma_f32_16x16x32_bf16 v[132:135], a[0:3], v[8:11], 0\n"
      "v_mfma_f32_16x16x32_bf16 v[136:139], a[4:7], v[8:11], 0\n"
      "v_mfma_f32_16x16x32_bf16 v[140:143], a[8:11], v[8:11], 0\n"
      "v_mfma_f32_16x16x32_bf16 v[144:147], a[12:15], v[8:11], 0\n"
      "v_mfma_f32_16x16x32_bf16 v[148:151], a[16:19], v[8:11], 0\n"
      "buffer_load_dwordx4 v[8:11], v229, s[12:15], s67 offen offset:0 sc1\n"
      "s_waitcnt vmcnt(11)\n"
      "v_mfma_f32_16x16x32_bf16 v[132:135], a[20:23], v[12:15], v[132:135]\n"
      "v_mfma_f32_16x16x32_bf16 v[136:139], a[24:27], v[12:15], v[136:139]\n"
      "v_mfma_f32_16x16x32_bf16 v[140:143], a[28:31], v[12:15], v[140:143]\n"
      "v_mfma_f32_16x16x32_bf16 v[144:147], a[32:35], v[12:15], v[144:147]\n"
      "v_mfma_f32_16x16x32_bf16 v[148:151], a[36:39], v[12:15], v[148:151]\n"
      "buffer_load_dwordx4 v[12:15], v229, s[12:15], s67 offen offset:1024 sc1\n"
      "s_waitcnt vmcnt(11)\n"
      "v_mfma_f32_16x16x32_bf16 v[132:135], a[40:43], v[16:19], v[132:135]\n"
      "v_mfma_f32_16x16x32_bf16 v[136:139], a[44:47], v[16:19], v[136:139]\n"
      "v_mfma_f32_16x16x32_bf16 v[140:143], a[48:51], v[16:19], v[140:143]\n"
      "v_mfma_f32_16x16x32_bf16 v[144:147], a[52:55], v[16:19], v[144:147]\n"
      "v_mfma_f32_16x16x32_bf16 v[148:151], a[56:59], v[16:19], v[148:151]\n"
      "s_waitcnt vmcnt(9)\n"
      "v_cmp_ne_u64 vcc, s[40:41], v[226:227]\n"
      "s_and_b32 vcc_lo, vcc_lo, 0xf\n"
      "s_cbranch_scc1 .Lk_slow_15\n"
      ".Lk_back_16:\n"
      "buffer_load_dwordx4 v[16:19], v207, s[12:15], s64 offen offset:0 sc1\n"
      "v_mfma_f32_16x16x32_bf16 v[132:135], a[60:63], v[20:23], v[132:135]\n"
      "v_mfma_f32_16x16x32_bf16 v[136:139], a[64:67], v[20:23], v[136:139]\n"
      "v_mfma_f32_16x16x32_bf16 v[140:143], a[68:71], v[20:23], v[140:143]\n"
      "ds_read_b128 a[212:215], v155 offset:0\n"
      "ds_read_b128 a[216:219], v155 offset:1024\n"
      "v_mfma_f32_16x16x32_bf16 v[144:147], a[72:75], v[20:23], v[144:147]\n"
      "ds_read_b128 a[220:223], v155 offset:2048\n"
      "ds_read_b128 a[224:227], v155 offset:3072\n"
      "v_mfma_f32_16x16x32_bf16 v[148:151], a[76:79], v[20:23], v[148:151]\n"
      "buffer_load_dwordx4 v[20:23], v207, s[12:15], s64 offen offset:1024 sc1\n"
      "s_waitcnt vmcnt(10)\n"
      "v_mfma_f32_16x16x32_bf16 v[132:135], a[80:83], v[24:27], v[132:135]\n"
      "ds_read_b128 a[228:231], v155 offset:4096\n"
      "ds_read_b128 a[232:235], v155 offset:5120\n"
      "v_mfma_f32_16x16x32_bf16 v[136:139], a[84:87], v[24:27], v[136:139]\n"
      "ds_read_b128 a[236:239], v155 offset:6144\n"
      "ds_read_b128 a[240:243], v155 offset:7168\n"
      "v_mfma_f32_16x16x32_bf16 v[140:143], a[88:91], v[24:27], v[140:143]\n"
      "ds_read_b128 v[56:59], v155 offset:8192\n"
      "ds_read_b128 v[60:63], v155 offset:9216\n"
      "v_mfma_f32_16x16x32_bf16 v[144:147], a[92:95], v[24:27], v[144:147]\n"
      "ds_read_b128 v[64:67], v155 offset:10240\n"
      "ds_read_b128 v[68:71], v155 offset:11264\n"
      "v_mfma_f32_16x16x32_bf16 v[148:151], a[96:99], v[24:27], v[148:151]\n"
      "buffer_load_dwordx4 v[24:27], v207, s[12:15], s64 offen offset:2048 sc1\n"
      "s_waitcnt vmcnt(10)\n"
      "v_mfma_f32_16x16x32_bf16 v[132:135], a[100:103], v[28:31], v[132:135]\n"
      "ds_read_b128 v[72:75], v155 offset:12288\n"
      "ds_read_b128 v[76:79], v155 offset:13312\n"
      "v_mfma_f32_16x16x32_bf16 v[136:139], a[104:107], v[28:31], v[136:139]\n"
      "ds_read_b128 v[80:83], v155 offset:14336\n"
      "ds_read_b128 v[84:87], v155 offset:15360\n"
      "v_mfma_f32_16x16x32_bf16 v[140:143], a[108:111], v[28:31], v[140:143]\n"
      "ds_read_b128 v[88:91], v155 offset:16384\n"
      "v_mfma_f32_16x16x32_bf16 v[144:147], a[112:115], v[28:31], v[144:147]\n"
      "v_mfma_f32_16x16x32_bf16 v[148:151], a[116:119], v[28:31], v[148:151]\n"
      "buffer_load_dwordx4 v[28:31], v207, s[12:15], s64 offen offset:3072 sc1\n"
      "s_waitcnt vmcnt(10)\n"
      "v_mfma_f32_16x16x32_bf16 v[160:163], a[0:3], v[32:35], v[160:163]\n"
      "v_mfma_f32_16x16x32_bf16 v[164:167], a[4:7], v[32:35], v[164:167]\n"
      "v_mfma_f32_16x16x32_bf16 v[168:171], a[8:11], v[32:35], v[168:171]\n"
      "v_mfma_f32_16x16x32_bf16 v[172:175], a[12:15], v[32:35], v[172:175]\n"
      "v_mfma_f32_16x16x32_bf16 v[176:179], a[16:19], v[32:35], v[176:179]\n"
      "buffer_load_dwordx4 v[32:35], v207, s[12:15], s65 offen offset:0 sc1\n"
      "s_waitcnt vmcnt(9)\n"
      "v_mfma_f32_16x16x32_bf16 v[160:163], a[20:23], v[36:39], v[160:163]\n"
      "v_mfma_f32_16x16x32_bf16 v[164:167], a[24:27], v[36:39], v[164:167]\n"
      "v_mfma_f32_16x16x32_bf16 v[168:171], a[28:31], v[36:39], v[168:171]\n"
      "s_waitcnt lgkmcnt(0)\n"
      "v_mfma_f32_16x16x32_bf16 v[172:175], a[32:35], v[36:39], v[172:175]\n"
      "v_mfma_f32_16x16x32_bf16 v[176:179], a[36:39], v[36:39], v[176:179]\n"
      "s_mov_b64 exec, 0xf\n"
      "buffer_load_dwordx2 v[226:227], v225, s[12:15], s62 offen sc1\n"
      "s_mov_b64 exec, -1\n"
      "buffer_load_dwordx4 v[36:39], v207, s[12:15], s65 offen offset:1024 sc1\n"
      "s_waitcnt vmcnt(10)\n"
      "v_mfma_f32_16x16x32_bf16 v[160:163], a[40:43], v[0:3], v[160:163]\n"
      "v_mfma_f32_16x16x32_bf16 v[164:167], a[44:47], v[0:3], v[164:167]\n"
      "v_mfma_f32_16x16x32_bf16 v[168:171], a[48:51], v[0:3], v[168:171]\n"
      "v_mfma_f32_16x16x32_bf16 v[172:175], a[52:55], v[0:3], v[172:175]\n"
      "v_mfma_f32_16x16x32_bf16 v[176:179], a[56:59], v[0:3], v[176:179]\n"
      "buffer_load_dwordx4 v[0:3], v207, s[12:15], s65 offen offset:2048 sc1\n"
      "s_waitcnt vmcnt(10)\n"
      "v_mfma_f32_16x16x32_bf16 v[160:163], a[60:63], v[4:7], v[160:163]\n"
      "v_mfma_f32_16x16x32_bf16 v[164:167], a[64:67], v[4:7], v[164:167]\n"
      "v_mfma_f32_16x16x32_bf16 v[168:171], a[68:71], v[4:7], v[168:171]\n"
      "v_mfma_f32_16x16x32_bf16 v[172:175], a[72:75], v[4:7], v[172:175]\n"
      "v_mfma_f32_16x16x32_bf16 v[176:179], a[76:79], v[4:7], v[176:179]\n"
      "buffer_load_dwordx4 v[4:7], v207, s[12:15], s65 offen offset:3072 sc1\n"
      "s_waitcnt vmcnt(10)\n"
      "v_mfma_f32_16x16x32_bf16 v[160:163], a[80:83], v[8:11], v[160:163]\n"
      "v_mfma_f32_16x16x32_bf16 v[164:167], a[84:87], v[8:11], v[164:167]\n"
      "v_mfma_f32_16x16x32_bf16 v[168:171], a[88:91], v[8:11], v[168:171]\n"
      "v_mfma_f32_16x16x32_bf16 v[172:175], a[92:95], v[8:11], v[172:175]\n"
      "v_mfma_f32_16x16x32_bf16 v[176:179], a[96:99], v[8:11], v[176:179]\n"
      "buffer_load_dwordx4 v[8:11], v207, s[12:15], s66 offen offset:0 sc1\n"
      "s_waitcnt vmcnt(10)\n"
      "v_mfma_f32_16x16x32_bf16 v[160:163], a[100:103], v[12:15], v[160:163]\n"
      "v_mfma_f32_16x16x32_bf16 v[164:167], a[104:107], v[12:15], v[164:167]\n"
      "v_mfma_f32_16x16x32_bf16 v[168:171], a[108:111], v[12:15], v[168:171]\n"
      "v_mfma_f32_16x16x32_bf16 v[172:175], a[112:115], v[12:15], v[172:175]\n"
      "v_mfma_f32_16x16x32_bf16 v[176:179], a[116:119], v[12:15], v[176:179]\n"
      "buffer_load_dwordx4 v[12:15], v207, s[12:15], s66 offen offset:1024 sc1\n"
      "s_waitcnt vmcnt(10)\n"
      "v_mfma_f32_16x16x32_bf16 v[92:95], a[120:123], v[16:19], v[92:95]\n"
      "v_mfma_f32_16x16x32_bf16 v[96:99], a[124:127], v[16:19], v[96:99]\n"
      "v_mfma_f32_16x16x32_bf16 v[100:103], a[128:131], v[16:19], v[100:103]\n"
      "v_mfma_f32_16x16x32_bf16 v[104:107], a[132:135], v[16:19], v[104:107]\n"
      "v_mfma_f32_16x16x32_bf16 v[108:111], a[136:139], v[16:19], v[108:111]\n"
      "buffer_load_dwordx4 v[16:19], v207, s[12:15], s66 offen offset:2048 sc1\n"
      "s_waitcnt vmcnt(10)\n"
      "v_mfma_f32_16x16x32_bf16 v[92:95], a[140:143], v[20:23], v[92:95]\n"
      "v_mfma_f32_16x16x32_bf16 v[96:99], a[144:147], v[20:23], v[96:99]\n"
      "v_mfma_f32_16x16x32_bf16 v[100:103], a[148:151], v[20:23], v[100:103]\n"
      "v_mfma_f32_16x16x32_bf16 v[104:107], a[152:155], v[20:23], v[104:107]\n"
      "v_mfma_f32_16x16x32_bf16 v[108:111], a[156:159], v[20:23], v[108:111]\n"
      "buffer_load_dwordx4 v[20:23], v207, s[12:15], s66 offen offset:3072 sc1\n"
      "s_waitcnt vmcnt(10)\n"
      "v_mfma_f32_16x16x32_bf16 v[92:95], a[160:163], v[24:27], v[92:95]\n"
      "v_mfma_f32_16x16x32_bf16 v[96:99], a[164:167], v[24:27], v[96:99]\n"
      "v_mfma_f32_16x16x32_bf16 v[100:103], a[168:171], v[24:27], v[100:103]\n"
      "v_mfma_f32_16x16x32_bf16 v[104:107], a[172:175], v[24:27], v[104:107]\n"
      "v_mfma_f32_16x16x32_bf16 v[108:111], a[176:179], v[24:27], v[108:111]\n"
      "s_waitcnt vmcnt(7)\n"
      "v_cmp_ne_u64 vcc, s[40:41], v[226:227]\n"
      "s_and_b32 vcc_lo, vcc_lo, 0xf\n"
      "s_cbranch_scc1 .Lk_slow_17\n"
      ".Lk_back_18:\n"
      "buffer_load_dwordx4 v[24:27], v208, s[12:15], s64 offen offset:0 sc1\n"
      "v_mfma_f32_16x16x32_bf16 v[92:95], a[180:183], v[28:31], v[92:95]\n"
      "v_mfma_f32_16x16x32_bf16 v[96:99], a[184:187], v[28:31], v[96:99]\n"
      "v_mfma_f32_16x16x32_bf16 v[100:103], a[188:191], v[28:31], v[100:103]\n"
      "v_mfma_f32_16x16x32_bf16 v[104:107], a[192:195], v[28:31], v[104:107]\n"
      "v_mfma_f32_16x16x32_bf16 v[108:111], a[196:199], v[28:31], v[108:111]\n"
      "buffer_load_dwordx4 v[28:31], v208, s[12:15], s64 offen offset:1024 sc1\n"
      "v_mfma_f32_16x16x32_bf16 v[112:115], a[120:123], v[32:35], v[112:115]\n"
      "v_mfma_f32_16x16x32_bf16 v[116:119], a[124:127], v[32:35], v[116:119]\n"
      "v_mfma_f32_16x16x32_bf16 v[120:123], a[128:131], v[32:35], v[120:123]\n"
      "v_mfma_f32_16x16x32_bf16 v[124:127], a[132:135], v[32:35], v[124:127]\n"
      "v_mfma_f32_16x16x32_bf16 v[128:131], a[136:139], v[32:35], v[128:131]\n"
      "buffer_load_dwordx4 v[32:35], v208, s[12:15], s64 offen offset:2048 sc1\n"
      "s_waitcnt vmcnt(9)\n"
      "v_mfma_f32_16x16x32_bf16 v[112:115], a[140:143], v[36:39], v[112:115]\n"
      "v_mfma_f32_16x16x32_bf16 v[116:119], a[144:147], v[36:39], v[116:119]\n"
      "v_mfma_f32_16x16x32_bf16 v[120:123], a[148:151], v[36:39], v[120:123]\n"
      "v_mfma_f32_16x16x32_bf16 v[124:127], a[152:155], v[36:39], v[124:127]\n"
      "v_mfma_f32_16x16x32_bf16 v[128:131], a[156:159], v[36:39], v[128:131]\n"
      "buffer_load_dwordx4 v[36:39], v208, s[12:15], s64 offen offset:3072 sc1\n"
      "s_waitcnt vmcnt(9)\n"
      "v_mfma_f32_16x16x32_bf16 v[112:115], a[160:163], v[0:3], v[112:115]\n"
      "v_mfma_f32_16x16x32_bf16 v[116:119], a[164:167], v[0:3], v[116:119]\n"
      "v_mfma_f32_16x16x32_bf16 v[120:123], a[168:171], v[0:3], v[120:123]\n"
      "v_mfma_f32_16x16x32_bf16 v[124:127], a[172:175], v[0:3], v[124:127]\n"
      "v_mfma_f32_16x16x32_bf16 v[128:131], a[176:179], v[0:3], v[128:131]\n"
      "buffer_load_dwordx4 v[0:3], v208, s[12:15], s65 offen offset:0 sc1\n"
      "s_waitcnt vmcnt(9)\n"
      "v_mfma_f32_16x16x32_bf16 v[112:115], a[180:183], v[4:7], v[112:115]\n"
      "v_mfma_f32_16x16x32_bf16 v[116:119], a[184:187], v[4:7], v[116:119]\n"
      "v_mfma_f32_16x16x32_bf16 v[120:123], a[188:191], v[4:7], v[120:123]\n"
      "v_mfma_f32_16x16x32_bf16 v[124:127], a[192:195], v[4:7], v[124:127]\n"
      "v_mfma_f32_16x16x32_bf16 v[128:131], a[196:199], v[4:7], v[128:131]\n"
      "buffer_load_dwordx4 v[4:7], v208, s[12:15], s65 offen offset:1024 sc1\n"
      "s_waitcnt vmcnt(9)\n"
      "v_mfma_f32_16x16x32_bf16 v[132:135], a[120:123], v[8:11], v[132:135]\n"
      "v_mfma_f32_16x16x32_bf16 v[136:139], a[124:127], v[8:11], v[136:139]\n"
      "v_mfma_f32_16x16x32_bf16 v[140:143], a[128:131], v[8:11], v[140:143]\n"
      "v_mfma_f32_16x16x32_bf16 v[144:147], a[132:135], v[8:11], v[144:147]\n"
      "v_mfma_f32_16x16x32_bf16 v[148:151], a[136:139], v[8:11], v[148:151]\n"
      "buffer_load_dwordx4 v[8:11], v208, s[12:15], s65 offen offset:2048 sc1\n"
      "s_waitcnt vmcnt(9)\n"
      "v_mfma_f32_16x16x32_bf16 v[132:135], a[140:143], v[12:15], v[132:135]\n"
      "v_mfma_f32_16x16x32_bf16 v[136:139], a[144:147], v[12:15], v[136:139]\n"
      "v_mfma_f32_16x16x32_bf16 v[140:143], a[148:151], v[12:15], v[140:143]\n"
      "v_mfma_f32_16x16x32_bf16 v[144:147], a[152:155], v[12:15], v[144:147]\n"
      "v_mfma_f32_16x16x32_bf16 v[148:151], a[156:159], v[12:15], v[148:151]\n"
      "buffer_load_dwordx4 v[12:15], v208, s[12:15], s65 offen offset:3072 sc1\n"
      "s_waitcnt vmcnt(9)\n"
      "v_mfma_f32_16x16x32_bf16 v[132:135], a[160:163], v[16:19], v[132:135]\n"
      "v_mfma_f32_16x16x32_bf16 v[136:139], a[164:167], v[16:19], v[136:139]\n"
      "v_mfma_f32_16x16x32_bf16 v[140:143], a[168:171], v[16:19], v[140:143]\n"
      "v_mfma_f32_16x16x32_bf16 v[144:147], a[172:175], v[16:19], v[144:147]\n"
      "v_mfma_f32_16x16x32_bf16 v[148:151], a[176:179], v[16:19], v[148:151]\n"
      "buffer_load_dwordx4 v[16:19], v208, s[12:15], s66 offen offset:0 sc1\n"
      "s_waitcnt vmcnt(9)\n"
      "v_mfma_f32_16x16x32_bf16 v[132:135], a[180:183], v[20:23], v[132:135]\n"
      "v_mfma_f32_16x16x32_bf16 v[136:139], a[184:187], v[20:23], v[136:139]\n"
      "v_mfma_f32_16x16x32_bf16 v[140:143], a[188:191], v[20:23], v[140:143]\n"
      "v_mfma_f32_16x16x32_bf16 v[144:147], a[192:195], v[20:23], v[144:147]\n"
      "v_mfma_f32_16x16x32_bf16 v[148:151], a[196:199], v[20:23], v[148:151]\n"
      "buffer_load_dwordx4 v[20:23], v208, s[12:15], s66 offen offset:1024 sc1\n"
      "s_waitcnt vmcnt(9)\n"
      "v_mfma_f32_16x16x32_bf16 v[92:95], a[200:203], v[24:27], v[92:95]\n"
      "v_mfma_f32_16x16x32_bf16 v[96:99], a[204:207], v[24:27], v[96:99]\n"
      "v_mfma_f32_16x16x32_bf16 v[100:103], a[208:211], v[24:27], v[100:103]\n"
      "v_mfma_f32_16x16x32_bf16 v[104:107], a[212:215], v[24:27], v[104:107]\n"
      "v_mfma_f32_16x16x32_bf16 v[108:111], a[216:219], v[24:27], v[108:111]\n"
      "buffer_load_dwordx4 v[24:27], v208, s[12:15], s66 offen offset:2048 sc1\n"
      "s_waitcnt vmcnt(9)\n"
      "v_mfma_f32_16x16x32_bf16 v[92:95], a[220:223], v[28:31], v[92:95]\n"
      "v_mfma_f32_16x16x32_bf16 v[96:99], a[224:227], v[28:31], v[96:99]\n"
      "v_mfma_f32_16x16x32_bf16 v[100:103], a[228:231], v[28:31], v[100:103]\n"
      "v_mfma_f32_16x16x32_bf16 v[104:107], a[232:235], v[28:31], v[104:107]\n"
      "v_mfma_f32_16x16x32_bf16 v[108:111], a[236:239], v[28:31], v[108:111]\n"
      "buffer_load_dwordx4 v[28:31], v208, s[12:15], s66 offen offset:3072 sc1\n"
      "s_waitcnt vmcnt(9)\n"
      "v_mfma_f32_16x16x32_bf16 v[92:95], a[240:243], v[32:35], v[92:95]\n"
      "v_mfma_f32_16x16x32_bf16 v[96:99], v[56:59], v[32:35], v[96:99]\n"
      "v_mfma_f32_16x16x32_bf16 v[100:103], v[60:63], v[32:35], v[100:103]\n"
      "v_mfma_f32_16x16x32_bf16 v[104:107], v[64:67], v[32:35], v[104:107]\n"
      "v_mfma_f32_16x16x32_bf16 v[108:111], v[68:71], v[32:35], v[108:111]\n"
      "buffer_load_dwordx4 v[32:35], v207, s[12:15], s67 offen offset:0 sc1\n"
      "s_waitcnt vmcnt(9)\n"
      "v_mfma_f32_16x16x32_bf16 v[92:95], v[72:75], v[36:39], v[92:95]\n"
      "v_mfma_f32_16x16x32_bf16 v[96:99], v[76:79], v[36:39], v[96:99]\n"
      "v_mfma_f32_16x16x32_bf16 v[100:103], v[80:83], v[36:39], v[100:103]\n"
      "v_mfma_f32_16x16x32_bf16 v[104:107], v[84:87], v[36:39], v[104:107]\n"
      "v_mfma_f32_16x16x32_bf16 v[108:111], v[88:91], v[36:39], v[108:111]\n"
      "buffer_load_dwordx4 v[36:39], v207, s[12:15], s67 offen offset:1024 sc1\n"
      "s_waitcnt vmcnt(9)\n"
      "v_mfma_f32_16x16x32_bf16 v[112:115], a[200:203], v[0:3], v[112:115]\n"
      "v_mfma_f32_16x16x32_bf16 v[116:119], a[204:207], v[0:3], v[116:119]\n"
      "v_mfma_f32_16x16x32_bf16 v[120:123], a[208:211], v[0:3], v[120:123]\n"
      "ds_write_b128 v202, v[92:95] offset:0\n"
      "v_mfma_f32_16x16x32_bf16 v[124:127], a[212:215], v[0:3], v[124:127]\n"
      "v_mfma_f32_16x16x32_bf16 v[128:131], a[216:219], v[0:3], v[128:131]\n"
      "buffer_load_dwordx4 v[0:3], v207, s[12:15], s67 offen offset:2048 sc1\n"
      "s_waitcnt vmcnt(9)\n"
      "v_mfma_f32_16x16x32_bf16 v[112:115], a[220:223], v[4:7], v[112:115]\n"
      "ds_write_b128 v202, v[96:99] offset:1024\n"
      "v_mfma_f32_16x16x32_bf16 v[116:119], a[224:227], v[4:7], v[116:119]\n"
      "v_mfma_f32_16x16x32_bf16 v[120:123], a[228:231], v[4:7], v[120:123]\n"
      "v_mfma_f32_16x16x32_bf16 v[124:127], a[232:235], v[4:7], v[124:127]\n"
      "ds_write_b128 v202, v[100:103] offset:2048\n"
      "v_mfma_f32_16x16x32_bf16 v[128:131], a[236:239], v[4:7], v[128:131]\n"
      "buffer_load_dwordx4 v[4:7], v207, s[12:15], s67 offen offset:3072 sc1\n"
      "s_waitcnt vmcnt(9)\n"
      "v_mfma_f32_16x16x32_bf16 v[112:115], a[240:243], v[8:11], v[112:115]\n"
      "v_mfma_f32_16x16x32_bf16 v[116:119], v[56:59], v[8:11], v[116:119]\n"
      "ds_write_b128 v202, v[104:107] offset:3072\n"
      "v_mfma_f32_16x16x32_bf16 v[120:123], v[60:63], v[8:11], v[120:123]\n"
      "v_mfma_f32_16x16x32_bf16 v[124:127], v[64:67], v[8:11], v[124:127]\n"
      "v_mfma_f32_16x16x32_bf16 v[128:131], v[68:71], v[8:11], v[128:131]\n"
      "buffer_load_dwordx4 v[8:11], v208, s[12:15], s67 offen offset:0 sc1\n"
      "ds_write_b128 v202, v[108:111] offset:4096\n"
      "s_waitcnt vmcnt(9)\n"
      "v_mfma_f32_16x16x32_bf16 v[112:115], v[72:75], v[12:15], v[112:115]\n"
      "v_mfma_f32_16x16x32_bf16 v[116:119], v[76:79], v[12:15], v[116:119]\n"
      "v_mfma_f32_16x16x32_bf16 v[120:123], v[80:83], v[12:15], v[120:123]\n"
      "v_mfma_f32_16x16x32_bf16 v[124:127], v[84:87], v[12:15], v[124:127]\n"
      "v_mfma_f32_16x16x32_bf16 v[128:131], v[88:91], v[12:15], v[128:131]\n"
      "buffer_load_dwordx4 v[12:15], v208, s[12:15], s67 offen offset:1024 sc1\n"
      "s_waitcnt vmcnt(9)\n"
      "v_mfma_f32_16x16x32_bf16 v[132:135], a[200:203], v[16:19], v[132:135]\n"
      "s_waitcnt lgkmcnt(0)\n"
      "s_barrier\n"
      "v_mfma_f32_16x16x32_bf16 v[136:139], a[204:207], v[16:19], v[136:139]\n"
      "ds_read_b128 v[92:95], v205 offset:0\n"
      "ds_read_b128 v[96:99], v205 offset:1024\n"
      "v_mfma_f32_16x16x32_bf16 v[140:143], a[208:211], v[16:19], v[140:143]\n"
      "ds_write_b128 v203, v[112:115] offset:0\n"
      "ds_read_b128 v[100:103], v205 offset:2048\n"
      "v_mfma_f32_16x16x32_bf16 v[144:147], a[212:215], v[16:19], v[144:147]\n"
      "ds_read_b128 v[104:107], v205 offset:3072\n"
      "ds_read_b128 v[108:111], v205 offset:4096\n"
      "v_mfma_f32_16x16x32_bf16 v[148:151], a[216:219], v[16:19], v[148:151]\n"
      "buffer_load_dwordx4 v[16:19], v208, s[12:15], s67 offen offset:2048 sc1\n"
      "s_waitcnt vmcnt(9)\n"
      "v_mfma_f32_16x16x32_bf16 v[132:135], a[220:223], v[20:23], v[132:135]\n"
      "ds_write_b128 v203, v[116:119] offset:1024\n"
      "v_mfma_f32_16x16x32_bf16 v[136:139], a[224:227], v[20:23], v[136:139]\n"
      "v_mfma_f32_16x16x32_bf16 v[140:143], a[228:231], v[20:23], v[140:143]\n"
      "v_mfma_f32_16x16x32_bf16 v[144:147], a[232:235], v[20:23], v[144:147]\n"
      "ds_write_b128 v203, v[120:123] offset:2048\n"
      "v_mfma_f32_16x16x32_bf16 v[148:151], a[236:239], v[20:23], v[148:151]\n"
      "buffer_load_dwordx4 v[20:23], v208, s[12:15], s67 offen offset:3072 sc1\n"
      "s_waitcnt vmcnt(9)\n"
      "v_mfma_f32_16x16x32_bf16 v[132:135], a[240:243], v[24:27], v[132:135]\n"
      "s_waitcnt lgkmcnt(2)\n"
      "v_add_f32 v160, v160, v92\n"
      "v_mfma_f32_16x16x32_bf16 v[136:139], v[56:59], v[24:27], v[136:139]\n"
      "ds_write_b128 v203, v[124:127] offset:3072\n"
      "v_add_f32 v161, v161, v93\n"
      "v_mfma_f32_16x16x32_bf16 v[140:143], v[60:63], v[24:27], v[140:143]\n"
      "s_mov_b64 exec, 0x1\n"
      "buffer_store_dwordx2 v[220:221], v159, s[12:15], 0 offen sc1\n"
      "s_mov_b64 exec, -1\n"
      "v_mfma_f32_16x16x32_bf16 v[144:147], v[64:67], v[24:27], v[144:147]\n"
      "v_add_f32 v162, v162, v94\n"
      "v_add_f32 v163, v163, v95\n"
      "v_mfma_f32_16x16x32_bf16 v[148:151], v[68:71], v[24:27], v[148:151]\n"
      "ds_write_b128 v203, v[128:131] offset:4096\n"
      "s_waitcnt vmcnt(9)\n"
      "v_mfma_f32_16x16x32_bf16 v[132:135], v[72:75], v[28:31], v[132:135]\n"
      "v_add_f32 v164, v164, v96\n"
      "v_add_f32 v165, v165, v97\n"
      "v_mfma_f32_16x16x32_bf16 v[136:139], v[76:79], v[28:31], v[136:139]\n"
      "v_add_f32 v166, v166, v98\n"
      "v_add_f32 v167, v167, v99\n"
      "v_mfma_f32_16x16x32_bf16 v[140:143], v[80:83], v[28:31], v[140:143]\n"
      "v_add_f32 v168, v168, v100\n"
      "v_add_f32 v169, v169, v101\n"
      "v_mfma_f32_16x16x32_bf16 v[144:147], v[84:87], v[28:31], v[144:147]\n"
      "v_add_f32 v170, v170, v102\n"
      "v_add_f32 v171, v171, v103\n"
      "v_mfma_f32_16x16x32_bf16 v[148:151], v[88:91], v[28:31], v[148:151]\n"
      "v_add_f32 v172, v172, v104\n"
      "v_add_f32 v173, v173, v105\n"
      "v_add_f32 v174, v174, v106\n"
      "v_add_f32 v175, v175, v107\n"
      "v_add_f32 v176, v176, v108\n"
      "v_add_f32 v177, v177, v109\n"
      "v_add_f32 v178, v178, v110\n"
      "v_add_f32 v179, v179, v111\n"
      "s_waitcnt vmcnt(8)\n"
      "v_mfma_f32_16x16x32_bf16 v[160:163], a[120:123], v[32:35], v[160:163]\n"
      "v_mfma_f32_16x16x32_bf16 v[164:167], a[124:127], v[32:35], v[164:167]\n"
      "v_mfma_f32_16x16x32_bf16 v[168:171], a[128:131], v[32:35], v[168:171]\n"
      "ds_write_b128 v204, v[132:135] offset:0\n"
      "v_mfma_f32_16x16x32_bf16 v[172:175], a[132:135], v[32:35], v[172:175]\n"
      "v_mfma_f32_16x16x32_bf16 v[176:179], a[136:139], v[32:35], v[176:179]\n"
      "s_waitcnt vmcnt(7)\n"
      "v_mfma_f32_16x16x32_bf16 v[160:163], a[140:143], v[36:39], v[160:163]\n"
      "ds_write_b128 v204, v[136:139] offset:1024\n"
      "v_mfma_f32_16x16x32_bf16 v[164:167], a[144:147], v[36:39], v[164:167]\n"
      "v_mfma_f32_16x16x32_bf16 v[168:171], a[148:151], v[36:39], v[168:171]\n"
      "v_mfma_f32_16x16x32_bf16 v[172:175], a[152:155], v[36:39], v[172:175]\n"
      "ds_write_b128 v204, v[140:143] offset:2048\n"
      "v_mfma_f32_16x16x32_bf16 v[176:179], a[156:159], v[36:39], v[176:179]\n"
      "s_waitcnt vmcnt(6)\n"
      "v_mfma_f32_16x16x32_bf16 v[160:163], a[160:163], v[0:3], v[160:163]\n"
      "v_mfma_f32_16x16x32_bf16 v[164:167], a[164:167], v[0:3], v[164:167]\n"
      "ds_write_b128 v204, v[144:147] offset:3072\n"
      "v_mfma_f32_16x16x32_bf16 v[168:171], a[168:171], v[0:3], v[168:171]\n"
      "v_mfma_f32_16x16x32_bf16 v[172:175], a[172:175], v[0:3], v[172:175]\n"
      "v_mfma_f32_16x16x32_bf16 v[176:179], a[176:179], v[0:3], v[176:179]\n"
      "ds_write_b128 v204, v[148:151] offset:4096\n"
      "s_waitcnt vmcnt(5)\n"
      "v_mfma_f32_16x16x32_bf16 v[160:163], a[180:183], v[4:7], v[160:163]\n"
      "s_waitcnt vmcnt(4)\n"
      "v_mfma_f32_16x16x32_bf16 v[160:163], a[200:203], v[8:11], v[160:163]\n"
      "s_waitcnt vmcnt(3)\n"
      "v_mfma_f32_16x16x32_bf16 v[160:163], a[220:223], v[12:15], v[160:163]\n"
      "s_waitcnt vmcnt(2)\n"
      "v_mfma_f32_16x16x32_bf16 v[160:163], a[240:243], v[16:19], v[160:163]\n"
      "s_waitcnt lgkmcnt(0)\n"
      "s_barrier\n"
      "s_waitcnt vmcnt(1)\n"
      "v_mfma_f32_16x16x32_bf16 v[160:163], v[72:75], v[20:23], v[160:163]\n"
      "ds_read_b128 v[112:115], v205 offset:5120\n"
      "ds_read_b128 v[132:135], v205 offset:10240\n"
      "v_mfma_f32_16x16x32_bf16 v[164:167], a[184:187], v[4:7], v[164:167]\n"
      "ds_read_b128 v[116:119], v205 offset:6144\n"
      "ds_read_b128 v[136:139], v205 offset:11264\n"
      "v_mfma_f32_16x16x32_bf16 v[164:167], a[204:207], v[8:11], v[164:167]\n"
      "ds_read_b128 v[120:123], v205 offset:7168\n"
      "ds_read_b128 v[140:143], v205 offset:12288\n"
      "v_mfma_f32_16x16x32_bf16 v[164:167], a[224:227], v[12:15], v[164:167]\n"
      "ds_read_b128 v[124:127], v205 offset:8192\n"
      "ds_read_b128 v[144:147], v205 offset:13312\n"
      "v_mfma_f32_16x16x32_bf16 v[164:167], v[56:59], v[16:19], v[164:167]\n"
      "ds_read_b128 v[128:131], v205 offset:9216\n"
      "ds_read_b128 v[148:151], v205 offset:14336\n"
      "v_mfma_f32_16x16x32_bf16 v[164:167], v[76:79], v[20:23], v[164:167]\n"
      "v_mfma_f32_16x16x32_bf16 v[168:171], a[188:191], v[4:7], v[168:171]\n"
      "v_mfma_f32_16x16x32_bf16 v[168:171], a[208:211], v[8:11], v[168:171]\n"
      "s_waitcnt lgkmcnt(8)\n"
      "v_add_f32 v112, v112, v132\n"
      "v_mfma_f32_16x16x32_bf16 v[168:171], a[228:231], v[12:15], v[168:171]\n"
      "v_add_f32 v113, v113, v133\n"
      "v_add_f32 v114, v114, v134\n"
      "v_mfma_f32_16x16x32_bf16 v[168:171], v[60:63], v[16:19], v[168:171]\n"
      "v_add_f32 v115, v115, v135\n"
      "s_waitcnt lgkmcnt(6)\n"
      "v_add_f32 v116, v116, v136\n"
      "v_mfma_f32_16x16x32_bf16 v[168:171], v[80:83], v[20:23], v[168:171]\n"
      "v_add_f32 v117, v117, v137\n"
      "v_add_f32 v118, v118, v138\n"
      "v_mfma_f32_16x16x32_bf16 v[172:175], a[192:195], v[4:7], v[172:175]\n"
      "v_add_f32 v119, v119, v139\n"
      "s_waitcnt lgkmcnt(4)\n"
      "v_add_f32 v120, v120, v140\n"
      "v_mfma_f32_16x16x32_bf16 v[172:175], a[212:215], v[8:11], v[172:175]\n"
      "v_add_f32 v121, v121, v141\n"
      "v_add_f32 v122, v122, v142\n"
      "v_mfma_f32_16x16x32_bf16 v[172:175], a[232:235], v[12:15], v[172:175]\n"
      "v_add_f32 v123, v123, v143\n"
      "s_waitcnt lgkmcnt(2)\n"
      "v_add_f32 v124, v124, v144\n"
      "v_mfma_f32_16x16x32_bf16 v[172:175], v[64:67], v[16:19], v[172:175]\n"
      "v_add_f32 v125, v125, v145\n"
      "v_add_f32 v126, v126, v146\n"
      "v_mfma_f32_16x16x32_bf16 v[172:175], v[84:87], v[20:23], v[172:175]\n"
      "v_add_f32 v127, v127, v147\n"
      "s_waitcnt lgkmcnt(0)\n"
      "v_add_f32 v128, v128, v148\n"
      "v_mfma_f32_16x16x32_bf16 v[176:179], a[196:199], v[4:7], v[176:179]\n"
      "v_add_f32 v129, v129, v149\n"
      "v_add_f32 v130, v130, v150\n"
      "v_mfma_f32_16x16x32_bf16 v[176:179], a[216:219], v[8:11], v[176:179]\n"
      "v_add_f32 v131, v131, v151\n"
      "v_add_f32 v160, v160, v112\n"
      "v_mfma_f32_16x16x32_bf16 v[176:179], a[236:239], v[12:15], v[176:179]\n"
      "v_add_f32 v161, v161, v113\n"
      "v_add_f32 v162, v162, v114\n"
      "v_mfma_f32_16x16x32_bf16 v[176:179], v[68:71], v[16:19], v[176:179]\n"
      "v_add_f32 v163, v163, v115\n"
      "v_cvt_pk_bf16_f32 v192, v160, v161\n"
      "v_mfma_f32_16x16x32_bf16 v[176:179], v[88:91], v[20:23], v[176:179]\n"
      "v_cvt_pk_bf16_f32 v193, v162, v163\n"
      "v_add_f32 v164, v164, v116\n"
      "v_add_f32 v165, v165, v117\n"
      "v_add_f32 v166, v166, v118\n"
      "v_add_f32 v167, v167, v119\n"
      "v_cvt_pk_bf16_f32 v194, v164, v165\n"
      "v_cvt_pk_bf16_f32 v195, v166, v167\n"
      "ds_write_b128 v153, v[192:195]\n"
      "v_add_f32 v168, v168, v120\n"
      "v_add_f32 v169, v169, v121\n"
      "v_add_f32 v170, v170, v122\n"
      "v_add_f32 v171, v171, v123\n"
      "v_cvt_pk_bf16_f32 v196, v168, v169\n"
      "v_cvt_pk_bf16_f32 v197, v170, v171\n"
      "v_add_f32 v172, v172, v124\n"
      "v_add_f32 v173, v173, v125\n"
      "v_add_f32 v174, v174, v126\n"
      "v_add_f32 v175, v175, v127\n"
      "v_cvt_pk_bf16_f32 v198, v172, v173\n"
      "v_cvt_pk_bf16_f32 v199, v174, v175\n"
      "ds_write_b128 v153, v[196:199] offset:64\n"
      "v_add_f32 v176, v176, v128\n"
      "v_add_f32 v177, v177, v129\n"
      "v_add_f32 v178, v178, v130\n"
      "v_add_f32 v179, v179, v131\n"
      "v_cvt_pk_bf16_f32 v200, v176, v177\n"
      "v_cvt_pk_bf16_f32 v201, v178, v179\n"
      "ds_write_b64 v154, v[200:201] offset:128\n"
      "ds_read_b128 v[0:3], v152 offset:0\n"
      "ds_read_b128 v[4:7], v152 offset:1024\n"
      "ds_read_b128 v[8:11], v152 offset:2048\n"
      "s_waitcnt lgkmcnt(2)\n"
      "buffer_store_dwordx4 v[0:3], v156, s[16:19], s92 offen\n"
      "s_waitcnt lgkmcnt(1)\n"
      "buffer_store_dwordx4 v[4:7], v157, s[16:19], s92 offen\n"
      "s_mov_b32 exec_hi, 0\n"
      "s_waitcnt lgkmcnt(0)\n"
      "buffer_store_dwordx4 v[8:11], v158, s[16:19], s92 offen\n"
      "s_mov_b32 exec_hi, -1\n"
      "s_endpgm\n"
      ".Lk_f2slow_4:\n"
      "s_cmp_eq_u32 s39, 0\n"
      "s_cbranch_scc1 .Lk_f2back_5\n"
      "s_sub_u32 s39, s39, 1\n"
      "buffer_load_dwordx4 v[232:235], v152, s[12:15], s72 offen sc1\n"
      "buffer_load_dwordx4 v[236:239], v153, s[12:15], s72 offen sc1\n"
      "buffer_load_dwordx4 v[228:231], v154, s[12:15], s72 offen sc1\n"
      "s_mov_b32 exec_lo, 0x3fff001f\n"
      "s_mov_b32 exec_hi, 0xf\n"
      "buffer_load_dwordx4 v[240:243], v204, s[12:15], s72 offen sc1\n"
      "s_mov_b64 exec, -1\n"
      "s_waitcnt vmcnt(0)\n"
      "v_bitop3_b32 v244, v197, v233, v237 bitop3:0x7e\n"
      "v_bitop3_b32 v245, v199, v235, v239 bitop3:0x7e\n"
      "v_bitop3_b32 v244, v244, v197, v229 bitop3:0xf6\n"
      "v_bitop3_b32 v245, v245, v199, v231 bitop3:0xf6\n"
      "v_bitop3_b32 v244, v244, v197, v241 bitop3:0xf6\n"
      "v_bitop3_b32 v245, v245, v199, v243 bitop3:0xf6\n"
      "v_cmp_ne_u64 vcc, 0, v[244:245]\n"
      "s_cbranch_vccnz .Lk_f2slow_4\n"
      "s_branch .Lk_f2back_5\n"
      ".Lk_slow_15:\n"
      "s_nop 4\n"
      "s_mov_b64 exec, 0xf\n"
      ".Lk_poll_19:\n"
      "s_sleep 1\n"
      "buffer_load_dwordx2 v[226:227], v225, s[12:15], s97 offen sc1\n"
      "s_waitcnt vmcnt(0)\n"
      "v_cmp_ne_u64 vcc, s[40:41], v[226:227]\n"
      "s_cbranch_vccz .Lk_polld_20\n"
      "s_cmp_eq_u32 s39, 0\n"
      "s_cbranch_scc1 .Lk_polld_20\n"
      "s_sub_u32 s39, s39, 1\n"
      "s_branch .Lk_poll_19\n"
      ".Lk_polld_20:\n"
      "s_mov_b64 exec, -1\n"
      "s_branch .Lk_back_16\n"
      ".Lk_slow_17:\n"
      "s_nop 4\n"
      "s_mov_b64 exec, 0xf\n"
      ".Lk_poll_21:\n"
      "s_sleep 1\n"
      "buffer_load_dwordx2 v[226:227], v225, s[12:15], s62 offen sc1\n"
      "s_waitcnt vmcnt(0)\n"
      "v_cmp_ne_u64 vcc, s[40:41], v[226:227]\n"
      "s_cbranch_vccz .Lk_polld_22\n"
      "s_cmp_eq_u32 s39, 0\n"
      "s_cbranch_scc1 .Lk_polld_22\n"
      "s_sub_u32 s39, s39, 1\n"
      "s_branch .Lk_poll_21\n"
      ".Lk_polld_22:\n"
      "s_mov_b64 exec, -1\n"
      "s_branch .Lk_back_18\n"
      ".Lk_end:\n"
      : "+{s[0:1]}"(ka), "+{s2}"(bid), "+{v0}"(tid), "+{s3}"(lb)
      :
      : "memory", "vcc", "scc", "v1", "v2", "v3", "v4", "v5", "v6", "v7", "v8", "v9", "v10", "v11", "v12", "v13", "v14", "v15", "v16",
        "v17", "v18", "v19", "v20", "v21", "v22", "v23", "v24", "v25", "v26", "v27", "v28", "v29", "v30", "v31", "v32",
        "v33", "v34", "v35", "v36", "v37", "v38", "v39", "v40", "v41", "v42", "v43", "v44", "v45", "v46", "v47", "v48",
        "v49", "v50", "v51", "v52", "v53", "v54", "v55", "v56", "v57", "v58", "v59", "v60", "v61", "v62", "v63", "v64",
        "v65", "v66", "v67", "v68", "v69", "v70", "v71", "v72", "v73", "v74", "v75", "v76", "v77", "v78", "v79", "v80",
        "v81", "v82", "v83", "v84", "v85", "v86", "v87", "v88", "v89", "v90", "v91", "v92", "v93", "v94", "v95", "v96",
        "v97", "v98", "v99", "v100", "v101", "v102", "v103", "v104", "v105", "v106", "v107", "v108", "v109", "v110", "v111", "v112",
        "v113", "v114", "v115", "v116", "v117", "v118", "v119", "v120", "v121", "v122", "v123", "v124", "v125", "v126", "v127", "v128",
        "v129", "v130", "v131", "v132", "v133", "v134", "v135", "v136", "v137", "v138", "v139", "v140", "v141", "v142", "v143", "v144",
        "v145", "v146", "v147", "v148", "v149", "v150", "v151", "v152", "v153", "v154", "v155", "v156", "v157", "v158", "v159", "v160",
        "v161", "v162", "v163", "v164", "v165", "v166", "v167", "v168", "v169", "v170", "v171", "v172", "v173", "v174", "v175", "v176",
        "v177", "v178", "v179", "v180", "v181", "v182", "v183", "v184", "v185", "v186", "v187", "v188", "v189", "v190", "v191", "v192",
        "v193", "v194", "v195", "v196", "v197", "v198", "v199", "v200", "v201", "v202", "v203", "v204", "v205", "v206", "v207", "v208",
        "v209", "v210", "v211", "v212", "v213", "v214", "v215", "v216", "v217", "v218", "v219", "v220", "v221", "v222", "v223", "v224",
        "v225", "v226", "v227", "v228", "v229", "v230", "v231", "v232", "v233", "v234", "v235", "v236", "v237", "v238", "v239", "v240",
        "v241", "v242", "v243", "v244", "v245", "v246", "v247", "v248", "v249", "v250", "v251", "v252", "v253", "v254", "v255", "a0",
        "a1", "a2", "a3", "a4", "a5", "a6", "a7", "a8", "a9", "a10", "a11", "a12", "a13", "a14", "a15", "a16",
        "a17", "a18", "a19", "a20", "a21", "a22", "a23", "a24", "a25", "a26", "a27", "a28", "a29", "a30", "a31", "a32",
        "a33", "a34", "a35", "a36", "a37", "a38", "a39", "a40", "a41", "a42", "a43", "a44", "a45", "a46", "a47", "a48",
        "a49", "a50", "a51", "a52", "a53", "a54", "a55", "a56", "a57", "a58", "a59", "a60", "a61", "a62", "a63", "a64",
        "a65", "a66", "a67", "a68", "a69", "a70", "a71", "a72", "a73", "a74", "a75", "a76", "a77", "a78", "a79", "a80",
        "a81", "a82", "a83", "a84", "a85", "a86", "a87", "a88", "a89", "a90", "a91", "a92", "a93", "a94", "a95", "a96",
        "a97", "a98", "a99", "a100", "a101", "a102", "a103", "a104", "a105", "a106", "a107", "a108", "a109", "a110", "a111", "a112",
        "a113", "a114", "a115", "a116", "a117", "a118", "a119", "a120", "a121", "a122", "a123", "a124", "a125", "a126", "a127", "a128",
        "a129", "a130", "a131", "a132", "a133", "a134", "a135", "a136", "a137", "a138", "a139", "a140", "a141", "a142", "a143", "a144",
        "a145", "a146", "a147", "a148", "a149", "a150", "a151", "a152", "a153", "a154", "a155", "a156", "a157", "a158", "a159", "a160",
        "a161", "a162", "a163", "a164", "a165", "a166", "a167", "a168", "a169", "a170", "a171", "a172", "a173", "a174", "a175", "a176",
        "a177", "a178", "a179", "a180", "a181", "a182", "a183", "a184", "a185", "a186", "a187", "a188", "a189", "a190", "a191", "a192",
        "a193", "a194", "a195", "a196", "a197", "a198", "a199", "a200", "a201", "a202", "a203", "a204", "a205", "a206", "a207", "a208",
        "a209", "a210", "a211", "a212", "a213", "a214", "a215", "a216", "a217", "a218", "a219", "a220", "a221", "a222", "a223", "a224",
        "a225", "a226", "a227", "a228", "a229", "a230", "a231", "a232", "a233", "a234", "a235", "a236", "a237", "a238", "a239", "a240",
        "a241", "a242", "a243", "a244", "a245", "a246", "a247", "a248", "a249", "a250", "a251", "a252", "a253", "a254", "a255", "s4",
        "s5", "s6", "s7", "s8", "s9", "s10", "s11", "s12", "s13", "s14", "s15", "s16", "s17", "s18", "s19", "s20",
        "s21", "s22", "s23", "s24", "s25", "s26", "s27", "s28", "s29", "s30", "s31", "s33", "s34", "s35", "s36", "s37",
        "s38", "s39", "s40", "s41", "s42", "s43", "s44", "s45", "s46", "s47", "s48", "s49", "s50", "s51", "s52", "s53",
        "s54", "s55", "s56", "s57", "s58", "s59", "s60", "s61", "s62", "s63", "s64", "s65", "s66", "s67", "s68", "s69",
        "s70", "s71", "s72", "s73", "s74", "s75", "s76", "s77", "s78", "s79", "s80", "s81", "s82", "s83", "s84", "s85",
        "s86", "s87", "s88", "s89", "s90", "s91", "s92", "s93", "s94", "s95", "s96", "s97", "s98", "s99");
#endif
}

extern "C" void kernel_launch(void* const* d_in, const int* in_sizes, int n_in, void* d_out, int out_size,
                              void* d_ws, size_t ws_size, hipStream_t stream) {
  const unsigned F = 0x00020000u;
  fused_block_kernel<<<256, 256, 0, stream>>>(
      d_in[0], 2048u * 640u * 2u, F, d_in[1], 640u * 1920u * 2u, F, d_ws, 21430272u, F, d_out, 2048u * 640u * 2u, F,
      d_in[2], 640u * 640u * 2u, F, d_in[4], 640u * 1728u * 2u, F, d_in[5], 1728u * 640u * 2u, F);
}
```
